```python
import torch

def _softmax(x, axis=-1):
    return torch.softmax(x, dim=axis)

def _gelu(x):
    return torch.nn.functional.gelu(x, approximate='tanh')

def _silu(x):
    return torch.nn.functional.silu(x)

def _sigmoid(x):
    return torch.sigmoid(x)

def _layernorm(x, axis=-1, eps=1e-05):
    m = x.mean(dim=axis, keepdim=True)
    v = x.var(dim=axis, keepdim=True, unbiased=False)
    return (x - m) / torch.sqrt(v + eps)

def _scatter_add(tgt, idx, src, axis=0):
    return tgt.clone().index_add_(axis, idx, src)

def _segment_sum(src, seg, n):
    out = torch.zeros((n,) + src.shape[1:], dtype=src.dtype)
    return out.index_add_(0, seg, src)

def _pad(x, pw):
    flat = tuple((v for pair in reversed(pw) for v in pair))
    return torch.nn.functional.pad(x, flat)
kernel_name = 'sequential_preLN__memory_aug_recurrent_mem_tr__glu_gated_bilinear__prompt'

def setup_inputs():

    _w = lambda a: (lambda t: t.to(torch.bfloat16) if t.is_floating_point() else t)(torch.as_tensor(a))

    def _A_setup():
        B, S, Dm = (1, 2048, 512)
        H, Dh = (32, 128)
        M, seg = (8, 32)
        seg = min(seg, S)
        S = S // seg * seg
        return {'x': _w((lambda *s: torch.randn(*s, dtype=torch.bfloat16) * 0.05)(B, S, Dm)), 'mem0': _w((lambda *s: torch.randn(*s, dtype=torch.bfloat16) * 0.2)(B, M, Dm)), 'Wq': _w((lambda *s: torch.randn(*s, dtype=torch.bfloat16) * 1.6)(Dm, H, Dh) * Dm ** (-0.5)), 'Wk': _w((lambda *s: torch.randn(*s, dtype=torch.bfloat16) * 1.6)(Dm, H, Dh) * Dm ** (-0.5)), 'Wv': _w((lambda *s: torch.randn(*s, dtype=torch.bfloat16) * 0.2)(Dm, H, Dh) * Dm ** (-0.5)), 'Wo': _w((lambda *s: torch.randn(*s, dtype=torch.bfloat16) * 0.2)(H, Dh, Dm) * (H * Dh) ** (-0.5)), 'seg_len': seg}
    _A_in = _A_setup()

    def _B_setup():
        B, S, D, F = (1, 2048, 512, 1792)
        return {'x': _w((lambda *s: torch.randn(*s, dtype=torch.bfloat16) * 0.05)(B, S, D)), 'w_gate': _w((lambda *s: torch.randn(*s, dtype=torch.bfloat16) * 0.2)(D, F) * D ** (-0.5)), 'w_up': _w((lambda *s: torch.randn(*s, dtype=torch.bfloat16) * 0.2)(D, F) * D ** (-0.5)), 'w_down': _w((lambda *s: torch.randn(*s, dtype=torch.bfloat16) * 0.2)(F, D) * F ** (-0.5))}
    _B_in = _B_setup()
    return {'A_x': _A_in['x'], 'A_mem0': _A_in['mem0'], 'A_Wq': _A_in['Wq'], 'A_Wk': _A_in['Wk'], 'A_Wv': _A_in['Wv'], 'A_Wo': _A_in['Wo'], 'A_seg_len': _A_in['seg_len'], 'B_x': _B_in['x'], 'B_w_gate': _B_in['w_gate'], 'B_w_up': _B_in['w_up'], 'B_w_down': _B_in['w_down']}

def reference(A_x, A_mem0, A_Wq, A_Wk, A_Wv, A_Wo, A_seg_len, B_x, B_w_gate, B_w_up, B_w_down):

    def _A_ref(x, mem0, Wq, Wk, Wv, Wo, seg_len):
        B, S, Dm = x.shape
        H, Dh = (Wq.shape[1], Wq.shape[2])
        M = mem0.shape[1]
        scale = Dh ** (-0.5)
        n_seg = S // seg_len
        L = M + seg_len
        idx = torch.arange(L)
        mask = (idx[:, None] < M) | (idx[:, None] >= idx[None, :])
        mem = mem0
        outs = []
        for s in range(n_seg):
            xs = x[:, s * seg_len:(s + 1) * seg_len, :].clone()
            h = torch.concatenate([mem, xs], axis=1)
            q = torch.einsum('btd,dhe->bhte', h, Wq)
            k = torch.einsum('btd,dhe->bhte', h, Wk)
            v = torch.einsum('btd,dhe->bhte', h, Wv)
            scores = torch.einsum('bhqe,bhke->bhqk', q, k) * scale
            scores = torch.where(mask[None, None], scores, -1000000000.0)
            p = _softmax(scores, axis=-1)
            o = torch.einsum('bhqk,bhke->bhqe', p, v)
            o = torch.einsum('bhte,hed->btd', o, Wo)
            mem = o[:, :M, :].clone()
            outs.append(o[:, M:, :])
        return torch.concatenate(outs, axis=1)

    def _B_ref(x, w_gate, w_up, w_down):
        g = torch.einsum('bsd,df->bsf', x, w_gate)
        u = torch.einsum('bsd,df->bsf', x, w_up)
        h = g * u
        return torch.einsum('bsf,fd->bsd', h, w_down)
    x = A_x
    h = x + _A_ref(_layernorm(x, axis=-1), A_mem0, A_Wq, A_Wk, A_Wv, A_Wo, A_seg_len)
    h2 = h + _B_ref(_layernorm(h, axis=-1), B_w_gate, B_w_up, B_w_down)
    return h2
```

```cpp
#include <hip/hip_runtime.h>
#include <hip/hip_bf16.h>
#include <stdint.h>

constexpr int S_ = 2048, D_ = 512, H_ = 32, DH_ = 128, M_ = 8, SEG_ = 32, NSEG_ = 64, F_ = 1792;

typedef short bf16x8 __attribute__((ext_vector_type(8)));
typedef short bf16x4 __attribute__((ext_vector_type(4)));
typedef float f32x4 __attribute__((ext_vector_type(4)));
typedef unsigned u32x4 __attribute__((ext_vector_type(4)));
typedef unsigned u32x2 __attribute__((ext_vector_type(2)));
typedef __bf16 bfv2 __attribute__((ext_vector_type(2)));

__device__ __forceinline__ unsigned pack2(float a, float b) {
    bfv2 v = {(__bf16)a, (__bf16)b};
    return __builtin_bit_cast(unsigned, v);
}
__device__ __forceinline__ float bf16r(float a) { return (float)(__bf16)a; }
__device__ __forceinline__ float bflo(unsigned u) { return __uint_as_float(u << 16); }
__device__ __forceinline__ float bfhi(unsigned u) { return __uint_as_float(u & 0xffff0000u); }
__device__ __forceinline__ bf16x8 pack8(f32x4 a, f32x4 b) {
    u32x4 r = {pack2(a[0], a[1]), pack2(a[2], a[3]), pack2(b[0], b[1]), pack2(b[2], b[3])};
    return __builtin_bit_cast(bf16x8, r);
}
__device__ __forceinline__ bf16x4 pack4(f32x4 a) {
    u32x2 r = {pack2(a[0], a[1]), pack2(a[2], a[3])};
    return __builtin_bit_cast(bf16x4, r);
}
#define MFMA32(a, b, c) __builtin_amdgcn_mfma_f32_16x16x32_bf16(a, b, c, 0, 0, 0)
#define MFMA16(a, b, c) __builtin_amdgcn_mfma_f32_16x16x16bf16_1k(a, b, c, 0, 0, 0)

constexpr size_t MB = 1024 * 1024;
constexpr size_t OFF_WQKV = 0;
constexpr size_t OFF_WO = OFF_WQKV + 12 * MB;
constexpr size_t OFF_WGU = OFF_WO + 4 * MB;
constexpr size_t OFF_WD = OFF_WGU + 4 * MB;
constexpr size_t OFF_XN = OFF_WD + 2 * MB;
constexpr size_t OFF_QF = OFF_XN + 2 * MB;
constexpr size_t OFF_KF = OFF_QF + 16 * MB;
constexpr size_t OFF_VF = OFF_KF + 16 * MB;
constexpr size_t OFF_KM = OFF_VF + 16 * MB;
constexpr size_t OFF_VM = OFF_KM + 8 * MB;
constexpr size_t OFF_OF = OFF_VM + 8 * MB;
constexpr size_t OFF_P1 = OFF_OF + 16 * MB;
constexpr size_t OFF_H = OFF_P1 + 32 * MB;
constexpr size_t OFF_HN = OFF_H + 2 * MB;
constexpr size_t OFF_AF = OFF_HN + 2 * MB;
constexpr size_t OFF_P2 = OFF_AF + 8 * MB;
constexpr size_t OFF_PART = OFF_P2 + 16 * MB;
constexpr size_t OFF_MEMV = OFF_PART + 1 * MB;
constexpr size_t WS_END = OFF_MEMV + 1 * MB;

__device__ __forceinline__ float wave_sum(float v) {
#pragma unroll
    for (int m = 1; m < 64; m <<= 1) v += __shfl_xor(v, m);
    return v;
}
__device__ __forceinline__ u32x4 ln_row(const float (&x)[8]) {
    float s = 0.f;
#pragma unroll
    for (int j = 0; j < 8; ++j) s += x[j];
    const float mean = wave_sum(s) * (1.0f / 512.0f);
    float q = 0.f;
#pragma unroll
    for (int j = 0; j < 8; ++j) { const float d = x[j] - mean; q += d * d; }
    const float var = wave_sum(q) * (1.0f / 512.0f);
    const float mb = bf16r(mean);
    const float vb = bf16r(var);
    const float den = bf16r(sqrtf(bf16r(vb + 1e-5f)));
    float y[8];
#pragma unroll
    for (int j = 0; j < 8; ++j) y[j] = bf16r(x[j] - mb) / den;
    u32x4 r = {pack2(y[0], y[1]), pack2(y[2], y[3]), pack2(y[4], y[5]), pack2(y[6], y[7])};
    return r;
}
__device__ __forceinline__ void store_row_frag(char* base, int row, int lane, u32x4 v) {
    const int tt = row >> 4, c = row & 15, ks = lane >> 2, g = lane & 3;
    *(u32x4*)(base + ((size_t)(tt * 16 + ks) * 64 + g * 16 + c) * 16) = v;
}

constexpr int NB_QKV = 3 * 1024, NB_WO = 1024, NB_GU = 2 * 448, NB_WD = 448, NB_LN = 512, NB_CLR = 96;
constexpr int NB_PREP = NB_QKV + NB_WO + NB_GU + NB_WD + NB_LN + NB_CLR;

__global__ void __launch_bounds__(256) prep_kernel(const unsigned short* __restrict__ x, const unsigned short* __restrict__ Wq,
                                                   const unsigned short* __restrict__ Wk, const unsigned short* __restrict__ Wv,
                                                   const unsigned short* __restrict__ Wo, const unsigned short* __restrict__ Wg,
                                                   const unsigned short* __restrict__ Wu, const unsigned short* __restrict__ Wd,
                                                   char* __restrict__ ws) {
    __shared__ __attribute__((aligned(16))) unsigned short t[32][72];
    int b = blockIdx.x;
    const int tid = threadIdx.x, wave = tid >> 6, lane = tid & 63;
    const unsigned short* src; int N, ks, nb, KS; bool perm = false; char* dst; int mode = 0;
    if (b < NB_QKV) {
        const int m = b >> 10; b &= 1023;
        src = m == 0 ? Wq : (m == 1 ? Wk : Wv); N = 4096; KS = 16; ks = b & 15; nb = b >> 4; dst = ws + OFF_WQKV + (size_t)m * 4 * MB;
    } else if (b < NB_QKV + NB_WO) {
        b -= NB_QKV; src = Wo; N = 512; KS = 128; ks = b & 127; nb = b >> 7; perm = true; dst = ws + OFF_WO;
    } else if (b < NB_QKV + NB_WO + NB_GU) {
        b -= NB_QKV + NB_WO; const int up = b >= 448; if (up) b -= 448;
        src = up ? Wu : Wg; N = 1792; KS = 16; ks = b & 15; nb = b >> 4; dst = ws + OFF_WGU; mode = 1 + up;
    } else if (b < NB_QKV + NB_WO + NB_GU + NB_WD) {
        b -= NB_QKV + NB_WO + NB_GU; src = Wd; N = 512; KS = 56; ks = b % 56; nb = b / 56; perm = true; dst = ws + OFF_WD;
    } else if (b < NB_QKV + NB_WO + NB_GU + NB_WD + NB_LN) {
        b -= NB_QKV + NB_WO + NB_GU + NB_WD;
        const int row = b * 4 + wave;
        const u32x4 v = *(const u32x4*)(x + (size_t)row * 512 + lane * 8);
        float f[8] = {bflo(v[0]), bfhi(v[0]), bflo(v[1]), bfhi(v[1]), bflo(v[2]), bfhi(v[2]), bflo(v[3]), bfhi(v[3])};
        store_row_frag(ws + OFF_XN, row, lane, ln_row(f));
        return;
    } else {
        b -= NB_QKV + NB_WO + NB_GU + NB_WD + NB_LN;
        const bool p = b < 64;
        char* d = p ? ws + OFF_PART + (size_t)b * 16384 : ws + OFF_MEMV + (size_t)(b - 64) * 16384;
        const unsigned f = p ? 0u : 0xFFFFFFFFu;
        const u32x4 v = {f, f, f, f};
#pragma unroll
        for (int i = 0; i < 4; ++i) *(u32x4*)(d + i * 4096 + tid * 16) = v;
        return;
    }
    {
        const int r = tid >> 3, cc = (tid & 7) * 8;
        const u32x4 v = *(const u32x4*)(src + (size_t)(ks * 32 + r) * N + nb * 64 + cc);
        *(u32x4*)&t[r][cc] = v;
    }
    __syncthreads();
    const int c = lane & 15, g = lane >> 4;
    unsigned short e[8];
#pragma unroll
    for (int j = 0; j < 8; ++j) {
        const int k = perm ? (j < 4 ? 4 * g + j : 16 + 4 * g + j - 4) : 8 * g + j;
        e[j] = t[k][wave * 16 + c];
    }
    int nt = nb * 4 + wave;
    if (mode) nt = (nt >> 1) * 4 + (mode == 2 ? 2 : 0) + (nt & 1);
    u32x4 o = {e[0] | ((unsigned)e[1] << 16), e[2] | ((unsigned)e[3] << 16), e[4] | ((unsigned)e[5] << 16), e[6] | ((unsigned)e[7] << 16)};
    *(u32x4*)(dst + ((size_t)(nt * KS + ks) * 64 + lane) * 16) = o;
}

enum { G_QKV = 0, G_OUTP = 1, G_FFN1 = 2, G_DOWN = 3 };

template <int MODE>
__global__ void __launch_bounds__(256, 1) gemm_kernel(char* __restrict__ ws) {
    constexpr int NKS = MODE == G_DOWN ? 14 : 16;
    constexpr int KST = MODE == G_OUTP ? 128 : (MODE == G_DOWN ? 56 : 16);
    constexpr int NT = MODE == G_FFN1 ? 4 : 2;
    constexpr int NIT = MODE == G_QKV ? 6 : (MODE == G_OUTP ? 2 : 1);
    __shared__ __attribute__((aligned(16))) char xl[8 * NKS * 1024];
    const int tid = threadIdx.x, wave = __builtin_amdgcn_readfirstlane(tid >> 6), lane = tid & 63;
    const int rb = blockIdx.x & 15, cb = blockIdx.x >> 4;
    int ks0 = 0, cg = cb;
    const char* xsrc; const char* wsrc;
    if (MODE == G_QKV) { xsrc = ws + OFF_XN; wsrc = ws + OFF_WQKV; }
    else if (MODE == G_OUTP) { xsrc = ws + OFF_OF; wsrc = ws + OFF_WO; ks0 = (cb & 7) * 16; cg = cb >> 3; }
    else if (MODE == G_FFN1) { xsrc = ws + OFF_HN; wsrc = ws + OFF_WGU; }
    else { xsrc = ws + OFF_AF; wsrc = ws + OFF_WD; ks0 = (cb & 3) * 14; cg = cb >> 2; }
    for (int i = wave; i < 8 * NKS; i += 4) {
        const int tt = i / NKS, ks = i % NKS;
        const u32x4 v = *(const u32x4*)(xsrc + ((size_t)((rb * 8 + tt) * KST + ks0 + ks) * 64 + lane) * 16);
        *(u32x4*)(xl + (size_t)i * 1024 + lane * 16) = v;
    }
    __syncthreads();
    const int c = lane & 15, g = lane >> 4;
    for (int it = 0; it < NIT; ++it) {
        int nt0; int mat = 0, head = 0;
        if (MODE == G_QKV) { const int ci = cg * 6 + it; mat = ci >> 5; head = ci & 31; nt0 = mat * 256 + head * 8 + wave * 2; }
        else if (MODE == G_OUTP) nt0 = (cg * 2 + it) * 8 + wave * 2;
        else if (MODE == G_FFN1) nt0 = (cg * 4 + wave) * 4;
        else nt0 = cg * 8 + wave * 2;
        f32x4 acc[NT][8];
#pragma unroll
        for (int t = 0; t < NT; ++t)
#pragma unroll
            for (int tt = 0; tt < 8; ++tt) acc[t][tt] = (f32x4){0.f, 0.f, 0.f, 0.f};
        const char* wp = wsrc + ((size_t)(nt0 * KST + ks0) * 64 + lane) * 16;
        const bool vmat = (MODE == G_QKV) && (mat == 2);
        if (vmat) {
#pragma unroll 4
            for (int ks = 0; ks < NKS; ++ks) {
                bf16x8 wf[NT];
#pragma unroll
                for (int t = 0; t < NT; ++t) wf[t] = *(const bf16x8*)(wp + (size_t)(t * KST + ks) * 1024);
#pragma unroll
                for (int tt = 0; tt < 8; ++tt) {
                    const bf16x8 xf = *(const bf16x8*)(xl + (tt * NKS + ks) * 1024 + lane * 16);
#pragma unroll
                    for (int t = 0; t < NT; ++t) acc[t][tt] = MFMA32(xf, wf[t], acc[t][tt]);
                }
            }
        } else {
#pragma unroll 4
            for (int ks = 0; ks < NKS; ++ks) {
                bf16x8 wf[NT];
#pragma unroll
                for (int t = 0; t < NT; ++t) wf[t] = *(const bf16x8*)(wp + (size_t)(t * KST + ks) * 1024);
#pragma unroll
                for (int tt = 0; tt < 8; ++tt) {
                    const bf16x8 xf = *(const bf16x8*)(xl + (tt * NKS + ks) * 1024 + lane * 16);
#pragma unroll
                    for (int t = 0; t < NT; ++t) acc[t][tt] = MFMA32(wf[t], xf, acc[t][tt]);
                }
            }
        }
        if (MODE == G_QKV) {
            if (mat == 2) {
#pragma unroll
                for (int sg = 0; sg < 4; ++sg)
#pragma unroll
                    for (int t = 0; t < 2; ++t) {
                        const int seg = rb * 4 + sg;
                        *(bf16x8*)(ws + OFF_VF + ((size_t)(((seg * 32 + head) * 4 + wave) * 2 + t) * 64 + lane) * 16) = pack8(acc[t][2 * sg], acc[t][2 * sg + 1]);
                    }
            } else {
                char* dst = ws + (mat == 0 ? OFF_QF : OFF_KF);
#pragma unroll
                for (int tt = 0; tt < 8; ++tt) {
                    const int seg = rb * 4 + (tt >> 1);
                    *(bf16x8*)(dst + ((size_t)(((seg * 32 + head) * 4 + wave) * 2 + (tt & 1)) * 64 + lane) * 16) = pack8(acc[0][tt], acc[1][tt]);
                }
            }
        } else if (MODE == G_OUTP || MODE == G_DOWN) {
            float* dst = (float*)(ws + (MODE == G_OUTP ? OFF_P1 : OFF_P2)) + (size_t)(MODE == G_OUTP ? (cb & 7) : (cb & 3)) * S_ * D_;
#pragma unroll
            for (int t = 0; t < 2; ++t)
#pragma unroll
                for (int tt = 0; tt < 8; ++tt)
                    *(f32x4*)(dst + (size_t)(rb * 128 + tt * 16 + c) * D_ + (nt0 + t) * 16 + g * 4) = acc[t][tt];
        } else {
            const int ag = cg * 4 + wave;
#pragma unroll
            for (int tt = 0; tt < 8; ++tt) {
                f32x4 a0, a1;
#pragma unroll
                for (int i = 0; i < 4; ++i) {
                    a0[i] = bf16r(acc[0][tt][i]) * bf16r(acc[2][tt][i]);
                    a1[i] = bf16r(acc[1][tt][i]) * bf16r(acc[NT - 1][tt][i]);
                }
                *(bf16x8*)(ws + OFF_AF + ((size_t)((rb * 8 + tt) * 56 + ag) * 64 + lane) * 16) = pack8(a0, a1);
            }
        }
    }
}

constexpr float SCALE_ = 0.08838834764831845f;
constexpr float LOG2E_ = 1.4426950408889634f;
constexpr unsigned SPIN_CAP = 1u << 17;

__device__ __forceinline__ float xor16(float v) { return __shfl_xor(v, 16); }
__device__ __forceinline__ float xor32(float v) { return __shfl_xor(v, 32); }

__global__ void __launch_bounds__(256, 1) chain_kernel(const unsigned short* mem0, char* ws) {
    __shared__ __attribute__((aligned(16))) char lds[128 * 1024 + 8192 + 12288 + 4096];
    char* const l_wo = lds;
    char* const l_mem = lds + 128 * 1024;
    char* const l_sx = l_mem + 8192;
    char* const l_ox = l_sx + 12288;
    const int tid = threadIdx.x, wave = __builtin_amdgcn_readfirstlane(tid >> 6), lane = tid & 63;
    const __amdgpu_buffer_rsrc_t prs = __builtin_amdgcn_make_buffer_rsrc(ws + OFF_PART, 0, 1024 * 1024, 0x00020000);
    const __amdgpu_buffer_rsrc_t mrs = __builtin_amdgcn_make_buffer_rsrc(ws + OFF_MEMV, 0, 64 * 8192, 0x00020000);

    if (blockIdx.x >= 32) {
        if (wave != 0) return;
        const int r = blockIdx.x - 32;
        const int t = r >> 2, gh = (r >> 1) & 1, rh = r & 1;
        const unsigned moff = (t >> 1) * 512 + (((t & 1) * 2 + gh) * 8 + 4 * rh) * 16 + (lane & 7) * 8;
        unsigned cap = SPIN_CAP;
        for (int s = 0; s < NSEG_ - 1; ++s) {
            const unsigned tag = ((s >> 1) + 1) & 1;
            unsigned base = ((s & 1) * 128 + r) * 4096 + lane * 16;
            u32x4 x[4];
            for (unsigned spins = 0;; ++spins) {
                unsigned bad = 0;
                asm volatile("" : "+v"(base)::"memory");
#pragma unroll
                for (int k = 0; k < 4; ++k) x[k] = __builtin_amdgcn_raw_buffer_load_b128(prs, base + k * 1024, 0, 16);
#pragma unroll
                for (int k = 0; k < 4; ++k) bad |= (x[k][0] ^ tag) | (x[k][1] ^ tag) | (x[k][2] ^ tag) | (x[k][3] ^ tag);
                if (__all((bad & 1u) == 0u)) break;
                if (spins >= cap) { cap = 0; break; }
            }
            f32x4 a = __builtin_bit_cast(f32x4, x[0]);
            a += __builtin_bit_cast(f32x4, x[1]);
            a += __builtin_bit_cast(f32x4, x[2]);
            a += __builtin_bit_cast(f32x4, x[3]);
#pragma unroll
            for (int m = 8; m < 64; m <<= 1) {
#pragma unroll
                for (int i = 0; i < 4; ++i) a[i] += __shfl_xor(a[i], m);
            }
            u32x2 o = {pack2(a[0], a[1]), pack2(a[2], a[3])};
            if (o[0] == 0xFFFFFFFFu) o[0] = 0x7FC07FC0u;
            if (o[1] == 0xFFFFFFFFu) o[1] = 0x7FC07FC0u;
            if (lane < 8) __builtin_amdgcn_raw_buffer_store_b64(o, mrs, (s + 1) * 8192 + moff, 0, 16);
        }
        return;
    }
    const int h = blockIdx.x;
    const int c = lane & 15, g = lane >> 4;
    bf16x8 wq[2][16], wk[2][16], wv[2][16];
    {
        const char* wb = ws + OFF_WQKV + ((size_t)((h * 8 + wave * 2) * 16) * 64 + lane) * 16;
#pragma unroll
        for (int t = 0; t < 2; ++t)
#pragma unroll
            for (int ks = 0; ks < 16; ++ks) {
                wq[t][ks] = *(const bf16x8*)(wb + (size_t)(t * 16 + ks) * 1024);
                wk[t][ks] = *(const bf16x8*)(wb + 4 * MB + (size_t)(t * 16 + ks) * 1024);
                wv[t][ks] = *(const bf16x8*)(wb + 8 * MB + (size_t)(t * 16 + ks) * 1024);
            }
    }
    for (int i = wave; i < 128; i += 4) {
        const int tile = i >> 2, kk = i & 3;
        *(u32x4*)(l_wo + i * 1024 + lane * 16) = *(const u32x4*)(ws + OFF_WO + ((size_t)(tile * 128 + h * 4 + kk) * 64 + lane) * 16);
    }
#pragma unroll
    for (int i = 0; i < 2; ++i) {
        const int idx = tid + i * 256;
        const int row = idx >> 6, ch = idx & 63;
        const u32x4 v = *(const u32x4*)(mem0 + row * 512 + ch * 8);
        *(u32x4*)(l_mem + (ch >> 2) * 512 + ((ch & 3) * 8 + row) * 16) = v;
    }
    __syncthreads();
    const char* kfp = ws + OFF_KF + ((size_t)((h * 4 + wave) * 2) * 64 + lane) * 16;
    const char* vfp = ws + OFF_VF + ((size_t)((h * 4 + wave) * 2) * 64 + lane) * 16;
    char* kmp = ws + OFF_KM + ((size_t)(h * 4 + wave) * 64 + lane) * 16;
    char* vmp = ws + OFF_VM + ((size_t)((h * 4 + wave) * 2) * 64 + lane) * 8;
    const int mrd = (g * 8 + (c & 7)) * 16;
    const unsigned poff = (unsigned)((((wave * 8) * 4 + (g >> 1) * 2 + ((c >> 2) & 1)) * 32 + h) * 128 + ((c & 3) * 2 + (g & 1)) * 16);

    unsigned cap = SPIN_CAP;
    for (int s = 0; s < NSEG_; ++s) {
        bf16x8 kf0, kf1, vf0, vf1;
        {
            const size_t so = (size_t)s * 32 * 8 * 1024;
            kf0 = *(const bf16x8*)(kfp + so); kf1 = *(const bf16x8*)(kfp + so + 1024);
            vf0 = *(const bf16x8*)(vfp + so); vf1 = *(const bf16x8*)(vfp + so + 1024);
        }
        f32x4 aq0 = {0, 0, 0, 0}, aq1 = {0, 0, 0, 0}, ak0 = {0, 0, 0, 0}, ak1 = {0, 0, 0, 0}, av0 = {0, 0, 0, 0}, av1 = {0, 0, 0, 0};
#pragma unroll
        for (int ks = 0; ks < 16; ++ks) {
            const bf16x8 mf = *(const bf16x8*)(l_mem + ks * 512 + mrd);
            aq0 = MFMA32(wq[0][ks], mf, aq0);
            aq1 = MFMA32(wq[1][ks], mf, aq1);
            ak0 = MFMA32(wk[0][ks], mf, ak0);
            ak1 = MFMA32(wk[1][ks], mf, ak1);
            av0 = MFMA32(mf, wv[0][ks], av0);
            av1 = MFMA32(mf, wv[1][ks], av1);
        }
        const bf16x8 qB = pack8(aq0, aq1);
        const bf16x8 kA = pack8(ak0, ak1);
        const bf16x4 vA0 = pack4(av0), vA1 = pack4(av1);
        *(bf16x8*)(kmp + (size_t)s * 32 * 4 * 1024) = kA;
        *(bf16x4*)(vmp + (size_t)s * 32 * 4 * 1024) = vA0;
        *(bf16x4*)(vmp + (size_t)s * 32 * 4 * 1024 + 512) = vA1;
        if (s == NSEG_ - 1) break;
        const f32x4 z = {0, 0, 0, 0};
        *(f32x4*)(l_sx + (wave * 3 + 0) * 1024 + lane * 16) = MFMA32(kf0, qB, z);
        *(f32x4*)(l_sx + (wave * 3 + 1) * 1024 + lane * 16) = MFMA32(kf1, qB, z);
        *(f32x4*)(l_sx + (wave * 3 + 2) * 1024 + lane * 16) = MFMA32(kA, qB, z);
        __syncthreads();
        f32x4 sc[3];
#pragma unroll
        for (int k = 0; k < 3; ++k) {
            sc[k] = *(const f32x4*)(l_sx + (0 * 3 + k) * 1024 + lane * 16);
            sc[k] += *(const f32x4*)(l_sx + (1 * 3 + k) * 1024 + lane * 16);
            sc[k] += *(const f32x4*)(l_sx + (2 * 3 + k) * 1024 + lane * 16);
            sc[k] += *(const f32x4*)(l_sx + (3 * 3 + k) * 1024 + lane * 16);
        }
        float mx = -3.0e38f;
#pragma unroll
        for (int k = 0; k < 3; ++k)
#pragma unroll
            for (int i = 0; i < 4; ++i) {
                float v = bf16r(bf16r(sc[k][i]) * SCALE_);
                if (k == 2 && g >= 2) v = -3.0e38f;
                sc[k][i] = v;
                mx = fmaxf(mx, v);
            }
        mx = fmaxf(mx, xor16(mx));
        mx = fmaxf(mx, xor32(mx));
        float sum = 0.f;
#pragma unroll
        for (int k = 0; k < 3; ++k)
#pragma unroll
            for (int i = 0; i < 4; ++i) {
                float e = exp2f((sc[k][i] - mx) * LOG2E_);
                if (k == 2 && g >= 2) e = 0.f;
                sc[k][i] = e;
                sum += e;
            }
        sum += xor16(sum);
        sum += xor32(sum);
        const float inv = 1.0f / sum;
#pragma unroll
        for (int k = 0; k < 3; ++k) sc[k] *= inv;
        const bf16x8 pB0 = pack8(sc[0], sc[1]);
        const bf16x4 pB1 = pack4(sc[2]);
        f32x4 o0 = MFMA32(vf0, pB0, z);
        f32x4 o1 = MFMA32(vf1, pB0, z);
        o0 = MFMA16(vA0, pB1, o0);
        o1 = MFMA16(vA1, pB1, o1);
        *(bf16x8*)(l_ox + wave * 1024 + lane * 16) = pack8(o0, o1);
        __syncthreads();
        bf16x8 oB[4];
#pragma unroll
        for (int kk = 0; kk < 4; ++kk) oB[kk] = *(const bf16x8*)(l_ox + kk * 1024 + lane * 16);
        const unsigned tag = ((s >> 1) + 1) & 1;
#pragma unroll
        for (int t = 0; t < 8; ++t) {
            f32x4 a = z;
#pragma unroll
            for (int kk = 0; kk < 4; ++kk)
                a = MFMA32(*(const bf16x8*)(l_wo + ((wave * 8 + t) * 4 + kk) * 1024 + lane * 16), oB[kk], a);
            u32x4 u = __builtin_bit_cast(u32x4, a);
            u = (u & ~1u) | tag;
            if (c < 8) __builtin_amdgcn_raw_buffer_store_b128(u, prs, (s & 1) * 128 * 4096 + poff + t * 4 * 4096, 0, 16);
        }
        {
            unsigned off = (s + 1) * 8192 + wave * 2048 + lane * 16;
            u32x4 y0, y1;
            for (unsigned spins = 0;; ++spins) {
                asm volatile("" : "+v"(off)::"memory");
                y0 = __builtin_amdgcn_raw_buffer_load_b128(mrs, off, 0, 16);
                y1 = __builtin_amdgcn_raw_buffer_load_b128(mrs, off + 1024, 0, 16);
                const bool ok = (y0[0] != 0xFFFFFFFFu) & (y0[1] != 0xFFFFFFFFu) & (y0[2] != 0xFFFFFFFFu) & (y0[3] != 0xFFFFFFFFu) &
                                (y1[0] != 0xFFFFFFFFu) & (y1[1] != 0xFFFFFFFFu) & (y1[2] != 0xFFFFFFFFu) & (y1[3] != 0xFFFFFFFFu);
                if (__all(ok)) break;
                if (spins >= cap) { cap = 0; break; }
            }
            *(u32x4*)(l_mem + wave * 2048 + lane * 16) = y0;
            *(u32x4*)(l_mem + wave * 2048 + 1024 + lane * 16) = y1;
        }
        __syncthreads();
    }
}

__global__ void __launch_bounds__(256) attn_kernel(char* __restrict__ ws) {
    const int tid = threadIdx.x, wave = tid >> 6, lane = tid & 63;
    const int id = blockIdx.x * 4 + wave;
    const int seg = id >> 5, h = id & 31;
    const int c = lane & 15, g = lane >> 4;
    const size_t fb = ((size_t)(seg * 32 + h) * 8 * 64 + lane) * 16;
    const f32x4 z = {0, 0, 0, 0};
    f32x4 sc[2][3];
#pragma unroll
    for (int qt = 0; qt < 2; ++qt)
#pragma unroll
        for (int kt = 0; kt < 3; ++kt) sc[qt][kt] = z;
#pragma unroll
    for (int w = 0; w < 4; ++w) {
        const bf16x8 q0 = *(const bf16x8*)(ws + OFF_QF + fb + (w * 2 + 0) * 1024);
        const bf16x8 q1 = *(const bf16x8*)(ws + OFF_QF + fb + (w * 2 + 1) * 1024);
        const bf16x8 k0 = *(const bf16x8*)(ws + OFF_KF + fb + (w * 2 + 0) * 1024);
        const bf16x8 k1 = *(const bf16x8*)(ws + OFF_KF + fb + (w * 2 + 1) * 1024);
        const bf16x8 km = *(const bf16x8*)(ws + OFF_KM + ((size_t)((seg * 32 + h) * 4 + w) * 64 + lane) * 16);
        sc[0][0] = MFMA32(k0, q0, sc[0][0]); sc[0][1] = MFMA32(k1, q0, sc[0][1]); sc[0][2] = MFMA32(km, q0, sc[0][2]);
        sc[1][0] = MFMA32(k0, q1, sc[1][0]); sc[1][1] = MFMA32(k1, q1, sc[1][1]); sc[1][2] = MFMA32(km, q1, sc[1][2]);
    }
    bf16x8 pB0[2]; bf16x4 pB1[2];
#pragma unroll
    for (int qt = 0; qt < 2; ++qt) {
        const int qi = qt * 16 + c;
        float mx = -3.0e38f;
#pragma unroll
        for (int kt = 0; kt < 3; ++kt)
#pragma unroll
            for (int i = 0; i < 4; ++i) {
                float v = bf16r(bf16r(sc[qt][kt][i]) * SCALE_);
                const bool valid = kt == 2 ? (g < 2) : (kt * 16 + 4 * g + i <= qi);
                v = valid ? v : -3.0e38f;
                sc[qt][kt][i] = v;
                mx = fmaxf(mx, v);
            }
        mx = fmaxf(mx, xor16(mx));
        mx = fmaxf(mx, xor32(mx));
        float sum = 0.f;
#pragma unroll
        for (int kt = 0; kt < 3; ++kt)
#pragma unroll
            for (int i = 0; i < 4; ++i) {
                const bool valid = kt == 2 ? (g < 2) : (kt * 16 + 4 * g + i <= qi);
                const float e = valid ? exp2f((sc[qt][kt][i] - mx) * LOG2E_) : 0.f;
                sc[qt][kt][i] = e;
                sum += e;
            }
        sum += xor16(sum);
        sum += xor32(sum);
        const float inv = 1.0f / sum;
#pragma unroll
        for (int kt = 0; kt < 3; ++kt) sc[qt][kt] *= inv;
        pB0[qt] = pack8(sc[qt][0], sc[qt][1]);
        pB1[qt] = pack4(sc[qt][2]);
    }
#pragma unroll
    for (int w = 0; w < 4; ++w) {
        const bf16x8 v0 = *(const bf16x8*)(ws + OFF_VF + fb + (w * 2 + 0) * 1024);
        const bf16x8 v1 = *(const bf16x8*)(ws + OFF_VF + fb + (w * 2 + 1) * 1024);
        const bf16x4 m0 = *(const bf16x4*)(ws + OFF_VM + ((size_t)(((seg * 32 + h) * 4 + w) * 2 + 0) * 64 + lane) * 8);
        const bf16x4 m1 = *(const bf16x4*)(ws + OFF_VM + ((size_t)(((seg * 32 + h) * 4 + w) * 2 + 1) * 64 + lane) * 8);
#pragma unroll
        for (int qt = 0; qt < 2; ++qt) {
            f32x4 o0 = MFMA32(v0, pB0[qt], z);
            f32x4 o1 = MFMA32(v1, pB0[qt], z);
            o0 = MFMA16(m0, pB1[qt], o0);
            o1 = MFMA16(m1, pB1[qt], o1);
            *(bf16x8*)(ws + OFF_OF + ((size_t)((seg * 2 + qt) * 128 + h * 4 + w) * 64 + lane) * 16) = pack8(o0, o1);
        }
    }
}

__global__ void __launch_bounds__(256) ln2_kernel(const unsigned short* __restrict__ x, char* __restrict__ ws) {
    const int tid = threadIdx.x, wave = tid >> 6, lane = tid & 63;
    const int row = blockIdx.x * 4 + wave;
    const float* p = (const float*)(ws + OFF_P1) + (size_t)row * 512 + lane * 8;
    f32x4 a0 = *(const f32x4*)p, a1 = *(const f32x4*)(p + 4);
#pragma unroll
    for (int k = 1; k < 8; ++k) { a0 += *(const f32x4*)(p + (size_t)k * S_ * D_); a1 += *(const f32x4*)(p + (size_t)k * S_ * D_ + 4); }
    const u32x4 v = *(const u32x4*)(x + (size_t)row * 512 + lane * 8);
    float f[8] = {bflo(v[0]), bfhi(v[0]), bflo(v[1]), bfhi(v[1]), bflo(v[2]), bfhi(v[2]), bflo(v[3]), bfhi(v[3])};
#pragma unroll
    for (int j = 0; j < 4; ++j) { f[j] = bf16r(f[j] + bf16r(a0[j])); f[4 + j] = bf16r(f[4 + j] + bf16r(a1[j])); }
    const u32x4 hv = {pack2(f[0], f[1]), pack2(f[2], f[3]), pack2(f[4], f[5]), pack2(f[6], f[7])};
    *(u32x4*)(ws + OFF_H + ((size_t)row * 512 + lane * 8) * 2) = hv;
    store_row_frag(ws + OFF_HN, row, lane, ln_row(f));
}

__global__ void __launch_bounds__(256) final_kernel(char* __restrict__ ws, unsigned short* __restrict__ out) {
    const size_t i = ((size_t)blockIdx.x * 256 + threadIdx.x) * 8;
    const float* p = (const float*)(ws + OFF_P2) + i;
    f32x4 a0 = *(const f32x4*)p, a1 = *(const f32x4*)(p + 4);
#pragma unroll
    for (int k = 1; k < 4; ++k) { a0 += *(const f32x4*)(p + (size_t)k * S_ * D_); a1 += *(const f32x4*)(p + (size_t)k * S_ * D_ + 4); }
    const u32x4 v = *(const u32x4*)(ws + OFF_H + i * 2);
    const u32x4 o = {pack2(bflo(v[0]) + bf16r(a0[0]), bfhi(v[0]) + bf16r(a0[1])), pack2(bflo(v[1]) + bf16r(a0[2]), bfhi(v[1]) + bf16r(a0[3])),
                     pack2(bflo(v[2]) + bf16r(a1[0]), bfhi(v[2]) + bf16r(a1[1])), pack2(bflo(v[3]) + bf16r(a1[2]), bfhi(v[3]) + bf16r(a1[3]))};
    *(u32x4*)(out + i) = o;
}

extern "C" void kernel_launch(void* const* d_in, const int* in_sizes, int n_in, void* d_out, int out_size, void* d_ws,
                              size_t ws_size, hipStream_t stream) {
    const unsigned short* x = (const unsigned short*)d_in[0];
    const unsigned short* mem0 = (const unsigned short*)d_in[1];
    const unsigned short* Wq = (const unsigned short*)d_in[2];
    const unsigned short* Wk = (const unsigned short*)d_in[3];
    const unsigned short* Wv = (const unsigned short*)d_in[4];
    const unsigned short* Wo = (const unsigned short*)d_in[5];
    const unsigned short* Wg = (const unsigned short*)d_in[8];
    const unsigned short* Wu = (const unsigned short*)d_in[9];
    const unsigned short* Wd = (const unsigned short*)d_in[10];
    char* ws = (char*)d_ws;
    prep_kernel<<<NB_PREP, 256, 0, stream>>>(x, Wq, Wk, Wv, Wo, Wg, Wu, Wd, ws);
    gemm_kernel<G_QKV><<<256, 256, 0, stream>>>(ws);
    chain_kernel<<<160, 256, 0, stream>>>(mem0, ws);
    attn_kernel<<<512, 256, 0, stream>>>(ws);
    gemm_kernel<G_OUTP><<<256, 256, 0, stream>>>(ws);
    ln2_kernel<<<512, 256, 0, stream>>>(x, ws);
    gemm_kernel<G_FFN1><<<224, 256, 0, stream>>>(ws);
    gemm_kernel<G_DOWN><<<256, 256, 0, stream>>>(ws);
    final_kernel<<<512, 256, 0, stream>>>(ws, (unsigned short*)d_out);
}
```

```cpp
#include <hip/hip_runtime.h>
#include <hip/hip_bf16.h>
#include <stdint.h>
#include <type_traits>

constexpr int S_ = 2048, D_ = 512, H_ = 32, DH_ = 128, M_ = 8, SEG_ = 32, NSEG_ = 64, F_ = 1792;

typedef short bf16x8 __attribute__((ext_vector_type(8)));
typedef short bf16x4 __attribute__((ext_vector_type(4)));
typedef float f32x4 __attribute__((ext_vector_type(4)));
typedef unsigned u32x4 __attribute__((ext_vector_type(4)));
typedef unsigned u32x2 __attribute__((ext_vector_type(2)));
typedef __bf16 bfv2 __attribute__((ext_vector_type(2)));

__device__ __forceinline__ unsigned pack2(float a, float b) {
    bfv2 v = {(__bf16)a, (__bf16)b};
    return __builtin_bit_cast(unsigned, v);
}
__device__ __forceinline__ float bf16r(float a) { return (float)(__bf16)a; }
__device__ __forceinline__ float bflo(unsigned u) { return __uint_as_float(u << 16); }
__device__ __forceinline__ float bfhi(unsigned u) { return __uint_as_float(u & 0xffff0000u); }
__device__ __forceinline__ bf16x8 pack8(f32x4 a, f32x4 b) {
    u32x4 r = {pack2(a[0], a[1]), pack2(a[2], a[3]), pack2(b[0], b[1]), pack2(b[2], b[3])};
    return __builtin_bit_cast(bf16x8, r);
}
__device__ __forceinline__ bf16x4 pack4(f32x4 a) {
    u32x2 r = {pack2(a[0], a[1]), pack2(a[2], a[3])};
    return __builtin_bit_cast(bf16x4, r);
}
#define MFMA32(a, b, c) __builtin_amdgcn_mfma_f32_16x16x32_bf16(a, b, c, 0, 0, 0)
__device__ __forceinline__ bf16x8 ext8(bf16x4 a) { bf16x8 r = {a[0], a[1], a[2], a[3], 0, 0, 0, 0}; return r; }
#define MFMA16(a, b, c) MFMA32(ext8(a), ext8(b), c)

constexpr size_t MB = 1024 * 1024;
constexpr size_t OFF_WQKV = 0;
constexpr size_t OFF_WO = OFF_WQKV + 12 * MB;
constexpr size_t OFF_WGU = OFF_WO + 4 * MB;
constexpr size_t OFF_WD = OFF_WGU + 4 * MB;
constexpr size_t OFF_XN = OFF_WD + 2 * MB;
constexpr size_t OFF_QF = OFF_XN + 2 * MB;
constexpr size_t OFF_KF = OFF_QF + 16 * MB;
constexpr size_t OFF_VF = OFF_KF + 16 * MB;
constexpr size_t OFF_KM = OFF_VF + 16 * MB;
constexpr size_t OFF_VM = OFF_KM + 8 * MB;
constexpr size_t OFF_OF = OFF_VM + 8 * MB;
constexpr size_t OFF_P1 = OFF_OF + 16 * MB;
constexpr size_t OFF_H = OFF_P1 + 32 * MB;
constexpr size_t OFF_HN = OFF_H + 2 * MB;
constexpr size_t OFF_AF = OFF_HN + 2 * MB;
constexpr size_t OFF_P2 = OFF_AF + 8 * MB;
constexpr size_t OFF_PART = OFF_P2 + 16 * MB;
constexpr size_t OFF_MEMV = OFF_PART + 1 * MB;
constexpr size_t OFF_CNT = OFF_MEMV + 4 * MB;
constexpr size_t WS_END = OFF_CNT + 1 * MB;

__device__ __forceinline__ float wave_sum(float v) {
#pragma unroll
    for (int m = 1; m < 64; m <<= 1) v += __shfl_xor(v, m);
    return v;
}
__device__ __forceinline__ u32x4 ln_row(const float (&x)[8]) {
    float s = 0.f;
#pragma unroll
    for (int j = 0; j < 8; ++j) s += x[j];
    const float mean = wave_sum(s) * (1.0f / 512.0f);
    float q = 0.f;
#pragma unroll
    for (int j = 0; j < 8; ++j) { const float d = x[j] - mean; q += d * d; }
    const float var = wave_sum(q) * (1.0f / 512.0f);
    const float mb = bf16r(mean);
    const float vb = bf16r(var);
    const float den = bf16r(sqrtf(bf16r(vb + 1e-5f)));
    float y[8];
#pragma unroll
    for (int j = 0; j < 8; ++j) y[j] = bf16r(x[j] - mb) / den;
    u32x4 r = {pack2(y[0], y[1]), pack2(y[2], y[3]), pack2(y[4], y[5]), pack2(y[6], y[7])};
    return r;
}
__device__ __forceinline__ void store_row_frag(char* base, int row, int lane, u32x4 v) {
    const int tt = row >> 4, c = row & 15, ks = lane >> 2, g = lane & 3;
    *(u32x4*)(base + ((size_t)(tt * 16 + ks) * 64 + g * 16 + c) * 16) = v;
}

constexpr int NB_QKV = 3 * 1024, NB_WO = 1024, NB_GU = 2 * 448, NB_WD = 448, NB_LN = 512, NB_CLR = 64 + 256 + 4;
constexpr int NB_PREP = NB_QKV + NB_WO + NB_GU + NB_WD + NB_LN + NB_CLR;

__global__ void __launch_bounds__(256) prep_kernel(const unsigned short* __restrict__ x, const unsigned short* __restrict__ Wq,
                                                   const unsigned short* __restrict__ Wk, const unsigned short* __restrict__ Wv,
                                                   const unsigned short* __restrict__ Wo, const unsigned short* __restrict__ Wg,
                                                   const unsigned short* __restrict__ Wu, const unsigned short* __restrict__ Wd,
                                                   char* __restrict__ ws) {
    __shared__ __attribute__((aligned(16))) unsigned short t[32][72];
    int b = blockIdx.x;
    const int tid = threadIdx.x, wave = tid >> 6, lane = tid & 63;
    const unsigned short* src; int N, ks, nb, KS; bool perm = false; char* dst; int mode = 0;
    if (b < NB_QKV) {
        const int m = b >> 10; b &= 1023;
        src = m == 0 ? Wq : (m == 1 ? Wk : Wv); N = 4096; KS = 16; ks = b & 15; nb = b >> 4; dst = ws + OFF_WQKV + (size_t)m * 4 * MB;
    } else if (b < NB_QKV + NB_WO) {
        b -= NB_QKV; src = Wo; N = 512; KS = 128; ks = b & 127; nb = b >> 7; perm = true; dst = ws + OFF_WO;
    } else if (b < NB_QKV + NB_WO + NB_GU) {
        b -= NB_QKV + NB_WO; const int up = b >= 448; if (up) b -= 448;
        src = up ? Wu : Wg; N = 1792; KS = 16; ks = b & 15; nb = b >> 4; dst = ws + OFF_WGU; mode = 1 + up;
    } else if (b < NB_QKV + NB_WO + NB_GU + NB_WD) {
        b -= NB_QKV + NB_WO + NB_GU; src = Wd; N = 512; KS = 56; ks = b % 56; nb = b / 56; perm = true; dst = ws + OFF_WD;
    } else if (b < NB_QKV + NB_WO + NB_GU + NB_WD + NB_LN) {
        b -= NB_QKV + NB_WO + NB_GU + NB_WD;
        const int row = b * 4 + wave;
        const u32x4 v = *(const u32x4*)(x + (size_t)row * 512 + lane * 8);
        float f[8] = {bflo(v[0]), bfhi(v[0]), bflo(v[1]), bfhi(v[1]), bflo(v[2]), bfhi(v[2]), bflo(v[3]), bfhi(v[3])};
        store_row_frag(ws + OFF_XN, row, lane, ln_row(f));
        return;
    } else {
        b -= NB_QKV + NB_WO + NB_GU + NB_WD + NB_LN;
        const bool p = b < 64 || b >= 320;
        char* d = b < 64 ? ws + OFF_PART + (size_t)b * 16384 : ws + OFF_MEMV + (size_t)(b - 64) * 16384;
        const unsigned f = p ? 0u : 0xFFFFFFFFu;
        const u32x4 v = {f, f, f, f};
#pragma unroll
        for (int i = 0; i < 4; ++i) *(u32x4*)(d + i * 4096 + tid * 16) = v;
        return;
    }
    {
        const int r = tid >> 3, cc = (tid & 7) * 8;
        const u32x4 v = *(const u32x4*)(src + (size_t)(ks * 32 + r) * N + nb * 64 + cc);
        *(u32x4*)&t[r][cc] = v;
    }
    __syncthreads();
    const int c = lane & 15, g = lane >> 4;
    unsigned short e[8];
#pragma unroll
    for (int j = 0; j < 8; ++j) {
        const int k = perm ? (j < 4 ? 4 * g + j : 16 + 4 * g + j - 4) : 8 * g + j;
        e[j] = t[k][wave * 16 + c];
    }
    int nt = nb * 4 + wave;
    if (mode) nt = (nt >> 1) * 4 + (mode == 2 ? 2 : 0) + (nt & 1);
    u32x4 o = {e[0] | ((unsigned)e[1] << 16), e[2] | ((unsigned)e[3] << 16), e[4] | ((unsigned)e[5] << 16), e[6] | ((unsigned)e[7] << 16)};
    *(u32x4*)(dst + ((size_t)(nt * KS + ks) * 64 + lane) * 16) = o;
}

enum { G_KV = 0, G_OUTP = 1, G_FFN1 = 2, G_DOWN = 3, G_Q = 4 };

__device__ __forceinline__ void st16_dev(const __amdgpu_buffer_rsrc_t rs, size_t off, u32x4 v) {
    __builtin_amdgcn_raw_buffer_store_b128(v, rs, (unsigned)off, 0, 16);
}

template <int MODE, int TT>
__device__ __forceinline__ void gemm_core(char* ws, char* xl, const __amdgpu_buffer_rsrc_t wrs, int tt0, int cb, int wave, int lane) {
    constexpr int NKS = MODE == G_DOWN ? 14 : 16;
    constexpr int KST = MODE == G_OUTP ? 128 : (MODE == G_DOWN ? 56 : 16);
    constexpr int NT = MODE == G_FFN1 ? 4 : 2;
    constexpr int NIT = MODE == G_KV ? 4 : 1;
    asm volatile("" : "+v"(lane));
    int ks0 = 0, cg = cb;
    const char* xsrc; const char* wsrc;
    if (MODE == G_KV || MODE == G_Q) { xsrc = ws + OFF_XN; wsrc = ws + OFF_WQKV; }
    else if (MODE == G_OUTP) { xsrc = ws + OFF_OF; wsrc = ws + OFF_WO; ks0 = (cb & 7) * 16; cg = cb >> 3; }
    else if (MODE == G_FFN1) { xsrc = ws + OFF_HN; wsrc = ws + OFF_WGU; }
    else { xsrc = ws + OFF_AF; wsrc = ws + OFF_WD; ks0 = (cb & 3) * 14; cg = cb >> 2; }
#pragma unroll
    for (int j = 0; j < (TT * NKS + 3) / 4; ++j) {
        const int i = j * 4 + wave;
        if (i < TT * NKS) {
            const int tt = i / NKS, ks = i % NKS;
            __builtin_amdgcn_global_load_lds((const unsigned*)(xsrc + ((size_t)((tt0 + tt) * KST + ks0 + ks) * 64 + lane) * 16),
                                             (unsigned*)(xl + (size_t)i * 1024 + lane * 16), 16, 0, 0);
        }
    }
    const int c = lane & 15, g = lane >> 4;
    auto tile0 = [&](int it) -> int {
        if (MODE == G_KV) { const int ci = 32 + cg * 4 + it; return (ci >> 5) * 256 + (ci & 31) * 8 + wave * 2; }
        else if (MODE == G_Q) return cg * 8 + wave * 2;
        else if (MODE == G_FFN1) return (cg * 4 + wave) * 4;
        else return cg * 8 + wave * 2;
    };
    constexpr int PD = NKS / 2;
    bf16x8 wr[PD][NT];
    {
        const char* wp0 = wsrc + ((size_t)(tile0(0) * KST + ks0) * 64 + lane) * 16;
#pragma unroll
        for (int ks = 0; ks < PD; ++ks)
#pragma unroll
            for (int t = 0; t < NT; ++t) wr[ks][t] = *(const bf16x8*)(wp0 + (size_t)(t * KST + ks) * 1024);
    }
    asm volatile("s_waitcnt vmcnt(0)" ::: "memory");
    __syncthreads();
#pragma unroll 1
    for (int it = 0; it < NIT; ++it) {
        asm volatile("" ::: "memory");
        const int nt0 = tile0(it);
        int mat = 0, head = 0;
        if (MODE == G_KV) { const int ci = 32 + cg * 4 + it; mat = ci >> 5; head = ci & 31; }
        f32x4 acc[NT][TT];
#pragma unroll
        for (int t = 0; t < NT; ++t)
#pragma unroll
            for (int tt = 0; tt < TT; ++tt) acc[t][tt] = (f32x4){0.f, 0.f, 0.f, 0.f};
        const char* wp = wsrc + ((size_t)(nt0 * KST + ks0) * 64 + lane) * 16;
        const char* wpn = wsrc + ((size_t)(tile0(it + 1 < NIT ? it + 1 : it) * KST + ks0) * 64 + lane) * 16;
        const bool vmat = (MODE == G_KV) && (mat == 2);
        auto kloop = [&](auto swapped) {
            bf16x8 xf[2][TT];
#pragma unroll
            for (int tt = 0; tt < TT; ++tt) xf[0][tt] = *(const bf16x8*)(xl + (tt * NKS) * 1024 + lane * 16);
#pragma unroll
            for (int ks = 0; ks < NKS; ++ks) {
                bf16x8 wf[NT];
#pragma unroll
                for (int t = 0; t < NT; ++t) {
                    wf[t] = wr[ks % PD][t];
                    if (NIT > 1 || ks + PD < NKS)
                        wr[ks % PD][t] = ks + PD < NKS ? *(const bf16x8*)(wp + (size_t)(t * KST + ks + PD) * 1024) : *(const bf16x8*)(wpn + (size_t)(t * KST + ks + PD - NKS) * 1024);
                }
                if (ks + 1 < NKS) {
#pragma unroll
                    for (int tt = 0; tt < TT; ++tt) xf[(ks + 1) & 1][tt] = *(const bf16x8*)(xl + (tt * NKS + ks + 1) * 1024 + lane * 16);
                }
                __builtin_amdgcn_sched_barrier(0);
#pragma unroll
                for (int tt = 0; tt < TT; ++tt)
#pragma unroll
                    for (int t = 0; t < NT; ++t) {
                        if constexpr (decltype(swapped)::value) acc[t][tt] = MFMA32(xf[ks & 1][tt], wf[t], acc[t][tt]);
                        else acc[t][tt] = MFMA32(wf[t], xf[ks & 1][tt], acc[t][tt]);
                    }
                __builtin_amdgcn_sched_barrier(0);
            }
        };
        if (vmat) kloop(std::true_type{}); else kloop(std::false_type{});
        if (MODE == G_KV) {
            if (mat == 2) {
#pragma unroll
                for (int sg = 0; sg < TT / 2; ++sg)
#pragma unroll
                    for (int t = 0; t < 2; ++t) {
                        const int seg = tt0 / 2 + sg;
                        *(bf16x8*)(ws + OFF_VF + ((size_t)(((seg * 32 + head) * 4 + wave) * 2 + t) * 64 + lane) * 16) = pack8(acc[t][2 * sg], acc[t][2 * sg + 1]);
                    }
            } else {
#pragma unroll
                for (int tt = 0; tt < TT; ++tt) {
                    const int seg = tt0 / 2 + (tt >> 1);
                    *(bf16x8*)(ws + OFF_KF + ((size_t)(((seg * 32 + head) * 4 + wave) * 2 + (tt & 1)) * 64 + lane) * 16) = pack8(acc[0][tt], acc[1][tt]);
                }
            }
        } else if (MODE == G_Q) {
            __syncthreads();
#pragma unroll
            for (int tt = 0; tt < TT; ++tt) *(bf16x8*)(xl + (tt * 4 + wave) * 1024 + lane * 16) = pack8(acc[0][tt], acc[1][tt]);
        } else if (MODE == G_OUTP || MODE == G_DOWN) {
            const size_t dst = (MODE == G_OUTP ? OFF_P1 : OFF_P2) + (size_t)(MODE == G_OUTP ? (cb & 7) : (cb & 3)) * S_ * D_ * 4;
#pragma unroll
            for (int t = 0; t < 2; ++t)
#pragma unroll
                for (int tt = 0; tt < TT; ++tt)
                    st16_dev(wrs, dst + ((size_t)((tt0 + tt) * 16 + c) * D_ + (nt0 + t) * 16 + g * 4) * 4, __builtin_bit_cast(u32x4, acc[t][tt]));
        } else {
            const int ag = cg * 4 + wave;
#pragma unroll
            for (int tt = 0; tt < TT; ++tt) {
                f32x4 a0, a1;
#pragma unroll
                for (int i = 0; i < 4; ++i) {
                    a0[i] = bf16r(acc[0][tt][i]) * bf16r(acc[2][tt][i]);
                    a1[i] = bf16r(acc[1][tt][i]) * bf16r(acc[NT - 1][tt][i]);
                }
                st16_dev(wrs, OFF_AF + ((size_t)((tt0 + tt) * 56 + ag) * 64 + lane) * 16, __builtin_bit_cast(u32x4, pack8(a0, a1)));
            }
        }
    }
}

__global__ void __launch_bounds__(256, 1) kv_kernel(char* ws) {
    __shared__ __attribute__((aligned(16))) char xl[128 * 1024];
    const int tid = threadIdx.x, wave = __builtin_amdgcn_readfirstlane(tid >> 6), lane = tid & 63;
    const __amdgpu_buffer_rsrc_t wrs = __builtin_amdgcn_make_buffer_rsrc(ws, 0, (unsigned)WS_END, 0x00020000);
    gemm_core<G_KV, 8>(ws, xl, wrs, (blockIdx.x >> 4) * 8, blockIdx.x & 15, wave, lane);
}

constexpr float SCALE_ = 0.08838834764831845f;
constexpr float LOG2E_ = 1.4426950408889634f;
constexpr unsigned SPIN_CAP = 1u << 17;

#define STAMP(i) do { } while (0)
__device__ __forceinline__ float max_x16(float v) {
    auto r = __builtin_amdgcn_permlane16_swap(__float_as_uint(v), __float_as_uint(v), false, false);
    return fmaxf(__uint_as_float(r[0]), __uint_as_float(r[1]));
}
__device__ __forceinline__ float max_x32(float v) {
    auto r = __builtin_amdgcn_permlane32_swap(__float_as_uint(v), __float_as_uint(v), false, false);
    return fmaxf(__uint_as_float(r[0]), __uint_as_float(r[1]));
}
__device__ __forceinline__ float add_x16(float v) {
    auto r = __builtin_amdgcn_permlane16_swap(__float_as_uint(v), __float_as_uint(v), false, false);
    return __uint_as_float(r[0]) + __uint_as_float(r[1]);
}
__device__ __forceinline__ float add_x32(float v) {
    auto r = __builtin_amdgcn_permlane32_swap(__float_as_uint(v), __float_as_uint(v), false, false);
    return __uint_as_float(r[0]) + __uint_as_float(r[1]);
}
__device__ __forceinline__ float add_x8(float v) {
    return v + __uint_as_float(__builtin_amdgcn_update_dpp(0u, __float_as_uint(v), 0x128, 0xf, 0xf, false));
}
#define MFMA_AA(acc, wa, b) asm volatile("v_mfma_f32_16x16x32_bf16 %0, %1, %2, %0" : "+v"(acc) : "a"(wa), "v"(b))
#define MFMA_AA0(acc, wa, b) asm volatile("v_mfma_f32_16x16x32_bf16 %0, %1, %2, 0" : "=&v"(acc) : "a"(wa), "v"(b))
#define MFMA_VV(acc, a, b) asm volatile("v_mfma_f32_16x16x32_bf16 %0, %1, %2, %0" : "+v"(acc) : "v"(a), "v"(b))
#define MFMA_VV0(acc, a, b) asm volatile("v_mfma_f32_16x16x32_bf16 %0, %1, %2, 0" : "=&v"(acc) : "v"(a), "v"(b))
#define NOP_IN2(a, b) asm volatile("s_nop 1" : "+v"(a), "+v"(b))
#define NOP_OUT2(a, b) asm volatile("s_nop 7\n\ts_nop 3" : "+v"(a), "+v"(b))
#define NOP_OUT4(a, b, c, d) asm volatile("s_nop 7\n\ts_nop 3" : "+v"(a), "+v"(b), "+v"(c), "+v"(d))

#define CPH 2
constexpr int NCW = 32 * CPH;
constexpr int TPW = 8 / CPH;
__device__ __forceinline__ float max_x8(float v) {
    return fmaxf(v, __uint_as_float(__builtin_amdgcn_update_dpp(0u, __float_as_uint(v), 0x128, 0xf, 0xf, false)));
}
__device__ __forceinline__ unsigned ror8(unsigned v) { return __builtin_amdgcn_update_dpp(0u, v, 0x128, 0xf, 0xf, false); }

#define HINTS 0
#define HPOS 2
#define DROPL 0
#define GDELAY 0
#define H2POS 1
#define HDELAY 0
#define NCOPY 8
#define RWV 2
constexpr int NRW = 128 / RWV;
constexpr int NWK = 128;
constexpr int NUNIT = 21;
__device__ __forceinline__ int unit_of_last_seg(int s) {
    if (s < 52) return (s & 3) == 3 ? (s >> 2) : -1;
    if (s < 60) return (s & 1) ? 13 + ((s - 52) >> 1) : -1;
    return 17 + (s - 60);
}
constexpr unsigned HINT1 = (unsigned)(OFF_CNT + 32768), HINT2 = (unsigned)(OFF_CNT + 32768 + 16384);
__device__ __forceinline__ void wait_hint(const __amdgpu_buffer_rsrc_t rs, unsigned off, unsigned want, unsigned cap) {
    for (unsigned spins = 0; spins <= cap; ++spins) {
        asm volatile("" : "+v"(off)::"memory");
        const unsigned v = __builtin_amdgcn_raw_buffer_load_b32(rs, off, 0, 16);
        if ((unsigned)__builtin_amdgcn_readfirstlane(v) >= want) break;
    }
}
typedef __attribute__((address_space(1))) unsigned gu32;
__device__ __forceinline__ gu32* cnt_ptr(char* ws, int u, int ph) { return (gu32*)(ws + OFF_CNT + (size_t)(u * 8 + ph) * 128); }
__device__ __forceinline__ void arrive(gu32* c, int tid) {
    asm volatile("s_waitcnt vmcnt(0)" ::: "memory");
    __syncthreads();
    if (tid == 0) __hip_atomic_fetch_add(c, 1u, __ATOMIC_RELAXED, __HIP_MEMORY_SCOPE_AGENT);
}
template <int SLP>
__device__ __forceinline__ void wait_for(gu32* c, unsigned target, int wave, unsigned& cap) {
    if (wave == 0) {
        for (unsigned spins = 0;; ++spins) {
            if ((unsigned)__builtin_amdgcn_readfirstlane(__hip_atomic_load(c, __ATOMIC_RELAXED, __HIP_MEMORY_SCOPE_AGENT)) >= target) break;
            if (spins >= cap) { cap = 0; break; }
            __builtin_amdgcn_s_sleep(SLP);
        }
        __builtin_amdgcn_fence(__ATOMIC_ACQUIRE, "agent");
        asm volatile("s_waitcnt vmcnt(0)" ::: "memory");
    }
    __syncthreads();
}

__device__ __forceinline__ void seg_attention(char* ws, const char* qf, const __amdgpu_buffer_rsrc_t wrs, int seg, int h, int lane) {
    asm volatile("" : "+v"(lane));
    const int c = lane & 15, g = lane >> 4;
    const size_t fb = ((size_t)(seg * 32 + h) * 8 * 64 + lane) * 16;
    const f32x4 z = {0, 0, 0, 0};
    f32x4 sc[2][3];
#pragma unroll
    for (int qt = 0; qt < 2; ++qt)
#pragma unroll
        for (int kt = 0; kt < 3; ++kt) sc[qt][kt] = z;
#pragma unroll
    for (int w = 0; w < 4; ++w) {
        const bf16x8 q0 = *(const bf16x8*)(qf + (0 * 4 + w) * 1024 + lane * 16);
        const bf16x8 q1 = *(const bf16x8*)(qf + (1 * 4 + w) * 1024 + lane * 16);
        const bf16x8 k0 = *(const bf16x8*)(ws + OFF_KF + fb + (w * 2 + 0) * 1024);
        const bf16x8 k1 = *(const bf16x8*)(ws + OFF_KF + fb + (w * 2 + 1) * 1024);
        const bf16x8 km = *(const bf16x8*)(ws + OFF_KM + ((size_t)((seg * 32 + h) * 4 + w) * 64 + lane) * 16);
        sc[0][0] = MFMA32(k0, q0, sc[0][0]); sc[0][1] = MFMA32(k1, q0, sc[0][1]); sc[0][2] = MFMA32(km, q0, sc[0][2]);
        sc[1][0] = MFMA32(k0, q1, sc[1][0]); sc[1][1] = MFMA32(k1, q1, sc[1][1]); sc[1][2] = MFMA32(km, q1, sc[1][2]);
    }
    bf16x8 pB0[2]; bf16x4 pB1[2];
#pragma unroll
    for (int qt = 0; qt < 2; ++qt) {
        const int qi = qt * 16 + c;
        float mx = -3.0e38f;
#pragma unroll
        for (int kt = 0; kt < 3; ++kt)
#pragma unroll
            for (int i = 0; i < 4; ++i) {
                float v = bf16r(bf16r(sc[qt][kt][i]) * SCALE_);
                const bool valid = kt == 2 ? (g < 2) : (kt * 16 + 4 * g + i <= qi);
                v = valid ? v : -3.0e38f;
                sc[qt][kt][i] = v;
                mx = fmaxf(mx, v);
            }
        mx = max_x32(max_x16(mx));
        float sum = 0.f;
#pragma unroll
        for (int kt = 0; kt < 3; ++kt)
#pragma unroll
            for (int i = 0; i < 4; ++i) {
                const bool valid = kt == 2 ? (g < 2) : (kt * 16 + 4 * g + i <= qi);
                const float e = valid ? __builtin_amdgcn_exp2f((sc[qt][kt][i] - mx) * LOG2E_) : 0.f;
                sc[qt][kt][i] = e;
                sum += e;
            }
        sum = add_x32(add_x16(sum));
        const float inv = __builtin_amdgcn_rcpf(sum);
#pragma unroll
        for (int kt = 0; kt < 3; ++kt) sc[qt][kt] *= inv;
        pB0[qt] = pack8(sc[qt][0], sc[qt][1]);
        pB1[qt] = pack4(sc[qt][2]);
    }
#pragma unroll
    for (int w = 0; w < 4; ++w) {
        const bf16x8 v0 = *(const bf16x8*)(ws + OFF_VF + fb + (w * 2 + 0) * 1024);
        const bf16x8 v1 = *(const bf16x8*)(ws + OFF_VF + fb + (w * 2 + 1) * 1024);
        const bf16x4 m0 = *(const bf16x4*)(ws + OFF_VM + ((size_t)(((seg * 32 + h) * 4 + w) * 2 + 0) * 64 + lane) * 8);
        const bf16x4 m1 = *(const bf16x4*)(ws + OFF_VM + ((size_t)(((seg * 32 + h) * 4 + w) * 2 + 1) * 64 + lane) * 8);
#pragma unroll
        for (int qt = 0; qt < 2; ++qt) {
            f32x4 o0 = MFMA32(v0, pB0[qt], z);
            f32x4 o1 = MFMA32(v1, pB0[qt], z);
            o0 = MFMA16(m0, pB1[qt], o0);
            o1 = MFMA16(m1, pB1[qt], o1);
            st16_dev(wrs, OFF_OF + ((size_t)((seg * 2 + qt) * 128 + h * 4 + w) * 64 + lane) * 16, __builtin_bit_cast(u32x4, pack8(o0, o1)));
        }
    }
}

template <int TT>
__device__ __forceinline__ void worker_unit(const unsigned short* x, unsigned short* out, char* ws, char* lds, const __amdgpu_buffer_rsrc_t wrs,
                                            int u, int seg0, int wg, int tid, int wave, int lane, unsigned& cap) {
    const int tt0 = seg0 * 2;
    gemm_core<G_Q, TT>(ws, lds, wrs, tt0, wg, wave, lane);
    wait_for<(TT > 2 ? 127 : 8)>(cnt_ptr(ws, u, 0), 32, wave, cap);
    if (wave < TT / 2) seg_attention(ws, lds + wave * 8192, wrs, seg0 + wave, wg, lane);
    arrive(cnt_ptr(ws, u, 1), tid);
    wait_for<(TT > 2 ? 32 : 4)>(cnt_ptr(ws, u, 1), 32, wave, cap);
    gemm_core<G_OUTP, TT>(ws, lds, wrs, tt0, wg, wave, lane);
    arrive(cnt_ptr(ws, u, 2), tid);
    wait_for<(TT > 2 ? 32 : 4)>(cnt_ptr(ws, u, 2), 32, wave, cap);
    const int lrow = wg * 4 + wave;
    const int row = tt0 * 16 + lrow;
    asm volatile("" : "+v"(lane));
    if (lrow < TT * 16) {
        const float* p = (const float*)(ws + OFF_P1) + (size_t)row * 512 + lane * 8;
        f32x4 a0 = *(const f32x4*)p, a1 = *(const f32x4*)(p + 4);
#pragma unroll
        for (int k = 1; k < 8; ++k) { a0 += *(const f32x4*)(p + (size_t)k * S_ * D_); a1 += *(const f32x4*)(p + (size_t)k * S_ * D_ + 4); }
        const u32x4 v = *(const u32x4*)(x + (size_t)row * 512 + lane * 8);
        float f[8] = {bflo(v[0]), bfhi(v[0]), bflo(v[1]), bfhi(v[1]), bflo(v[2]), bfhi(v[2]), bflo(v[3]), bfhi(v[3])};
#pragma unroll
        for (int j = 0; j < 4; ++j) { f[j] = bf16r(f[j] + bf16r(a0[j])); f[4 + j] = bf16r(f[4 + j] + bf16r(a1[j])); }
        const u32x4 hv = {pack2(f[0], f[1]), pack2(f[2], f[3]), pack2(f[4], f[5]), pack2(f[6], f[7])};
        st16_dev(wrs, OFF_H + ((size_t)row * 512 + lane * 8) * 2, hv);
        st16_dev(wrs, OFF_HN + ((size_t)((row >> 4) * 16 + (lane >> 2)) * 64 + (lane & 3) * 16 + (row & 15)) * 16, ln_row(f));
    }
    arrive(cnt_ptr(ws, u, 3), tid);
    wait_for<(TT > 2 ? 32 : 4)>(cnt_ptr(ws, u, 3), 32, wave, cap);
    if (wg < 14) gemm_core<G_FFN1, TT>(ws, lds, wrs, tt0, wg, wave, lane);
    arrive(cnt_ptr(ws, u, 4), tid);
    wait_for<(TT > 2 ? 32 : 4)>(cnt_ptr(ws, u, 4), 32, wave, cap);
    if (wg < 16) gemm_core<G_DOWN, TT>(ws, lds, wrs, tt0, wg, wave, lane);
    arrive(cnt_ptr(ws, u, 5), tid);
    wait_for<(TT > 2 ? 32 : 4)>(cnt_ptr(ws, u, 5), 32, wave, cap);
    asm volatile("" : "+v"(lane));
    if (lrow < TT * 16) {
        const size_t i = (size_t)row * 512 + lane * 8;
        const float* p = (const float*)(ws + OFF_P2) + i;
        f32x4 a0 = *(const f32x4*)p, a1 = *(const f32x4*)(p + 4);
#pragma unroll
        for (int k = 1; k < 4; ++k) { a0 += *(const f32x4*)(p + (size_t)k * S_ * D_); a1 += *(const f32x4*)(p + (size_t)k * S_ * D_ + 4); }
        const u32x4 v = *(const u32x4*)(ws + OFF_H + i * 2);
        const u32x4 o = {pack2(bflo(v[0]) + bf16r(a0[0]), bfhi(v[0]) + bf16r(a0[1])), pack2(bflo(v[1]) + bf16r(a0[2]), bfhi(v[1]) + bf16r(a0[3])),
                         pack2(bflo(v[2]) + bf16r(a1[0]), bfhi(v[2]) + bf16r(a1[1])), pack2(bflo(v[3]) + bf16r(a1[2]), bfhi(v[3]) + bf16r(a1[3]))};
        *(u32x4*)(out + i) = o;
    }
}

__global__ void __launch_bounds__(256, 1) chain_kernel(const unsigned short* mem0, const unsigned short* x, unsigned short* out, char* ws) {
    __shared__ __attribute__((aligned(16))) char lds[CPH > 1 ? 128 * 1024 : 128 * 1024 + 24576];
    char* const l_mem = lds;
    char* const l_sx = l_mem + 8192;
    char* const l_ox = l_sx + 12288;
    char* const l_wo = l_ox + 4096;
    const int wave = __builtin_amdgcn_readfirstlane(threadIdx.x >> 6);
    const int lane = __builtin_amdgcn_mbcnt_hi(~0u, __builtin_amdgcn_mbcnt_lo(~0u, 0u));
    const int tid = wave * 64 + lane;
    const __amdgpu_buffer_rsrc_t prs = __builtin_amdgcn_make_buffer_rsrc(ws + OFF_PART, 0, 1024 * 1024, 0x00020000);
    const __amdgpu_buffer_rsrc_t mrs = __builtin_amdgcn_make_buffer_rsrc(ws + OFF_MEMV, 0, 64 * 65536, 0x00020000);

    const __amdgpu_buffer_rsrc_t wrs = __builtin_amdgcn_make_buffer_rsrc(ws, 0, (unsigned)WS_END, 0x00020000);
    if (blockIdx.x >= NCW + NRW) {
        const int wk = blockIdx.x - (NCW + NRW), grp = wk >> 5, wg = wk & 31;
        unsigned cap = SPIN_CAP;
#pragma unroll 1
        for (int u = grp; u < 13; u += 4) worker_unit<8>(x, out, ws, lds, wrs, u, 4 * u, wg, tid, wave, lane, cap);
        {
            const int u = 13 + ((grp + 3) & 3);
            worker_unit<4>(x, out, ws, lds, wrs, u, 52 + 2 * (u - 13), wg, tid, wave, lane, cap);
        }
        if (grp == 1 || grp == 2) worker_unit<2>(x, out, ws, lds, wrs, 16 + grp, 59 + grp, wg, tid, wave, lane, cap);
        return;
    }
    if (blockIdx.x >= NCW) {
        const int rw = blockIdx.x - NCW;
        unsigned cap = SPIN_CAP;
        if (wave < RWV) {
        const int r = rw * RWV + wave;
        const int t = r >> 2, gh = (r >> 1) & 1, rh = r & 1;
        const unsigned moff = (lane >> 3) < NCOPY ? (unsigned)((lane >> 3) * 8192 + (t >> 1) * 512 + (((t & 1) * 2 + gh) * 8 + 4 * rh) * 16 + (lane & 7) * 8) : 0x40000000u;
        for (int s = 0; s < NSEG_ - 1; ++s) {
            const unsigned tag = ((s >> 1) + 1) & 1;
            unsigned base = ((s & 1) * 128 + r) * 4096 + lane * 16;
            u32x4 x[4];
            for (unsigned spins = 0;; ++spins) {
                unsigned bad = 0;
                asm volatile("" : "+v"(base)::"memory");
#pragma unroll
                for (int k = 0; k < 4; ++k) x[k] = __builtin_amdgcn_raw_buffer_load_b128(prs, base + k * 1024, 0, 16);
#pragma unroll
                for (int k = 0; k < 4; ++k) bad |= (x[k][0] ^ tag) | (x[k][1] ^ tag) | (x[k][2] ^ tag) | (x[k][3] ^ tag);
                if (__all((bad & 1u) == 0u)) break;
                if (spins >= cap) { cap = 0; break; }
            }
            STAMP(0);
            f32x4 a = (__builtin_bit_cast(f32x4, x[0]) + __builtin_bit_cast(f32x4, x[1])) + (__builtin_bit_cast(f32x4, x[2]) + __builtin_bit_cast(f32x4, x[3]));
#pragma unroll
            for (int i = 0; i < 4; ++i) a[i] = add_x32(add_x16(add_x8(a[i])));
            u32x2 o = {pack2(a[0], a[1]), pack2(a[2], a[3])};
            if (o[0] == 0xFFFFFFFFu) o[0] = 0x7FC07FC0u;
            if (o[1] == 0xFFFFFFFFu) o[1] = 0x7FC07FC0u;
            __builtin_amdgcn_raw_buffer_store_b64(o, mrs, (s + 1) * 65536 + moff, 0, 16);
            STAMP(1);
        }
        }
        __syncthreads();
        if (rw < 64) worker_unit<2>(x, out, ws, lds, wrs, 19 + (rw >> 5), 62 + (rw >> 5), rw & 31, tid, wave, lane, cap);
        return;
    }
    const int h = blockIdx.x & 31, part = blockIdx.x >> 5;
    const int c = lane & 15, g = lane >> 4;
    bf16x8 wq[2][16], wk[2][16], wv[2][16];
    {
        const char* wb = ws + OFF_WQKV + ((size_t)((h * 8 + wave * 2) * 16) * 64 + lane) * 16;
#pragma unroll
        for (int t = 0; t < 2; ++t)
#pragma unroll
            for (int ks = 0; ks < 16; ++ks) {
                wq[t][ks] = *(const bf16x8*)(wb + (size_t)(t * 16 + ks) * 1024);
                wk[t][ks] = *(const bf16x8*)(wb + 4 * MB + (size_t)(t * 16 + ks) * 1024);
                wv[t][ks] = *(const bf16x8*)(wb + 8 * MB + (size_t)(t * 16 + ks) * 1024);
            }
    }
    for (int i = wave; i < 128 / CPH; i += 4) {
        const int tile = part * (32 / CPH) + (i >> 2), kk = i & 3;
        *(u32x4*)(l_wo + i * 1024 + lane * 16) = *(const u32x4*)(ws + OFF_WO + ((size_t)(tile * 128 + h * 4 + kk) * 64 + lane) * 16);
    }
#pragma unroll
    for (int i = 0; i < 2; ++i) {
        const int idx = tid + i * 256;
        const int row = idx >> 6, ch = idx & 63;
        const u32x4 v = *(const u32x4*)(mem0 + row * 512 + ch * 8);
        *(u32x4*)(l_mem + (ch >> 2) * 512 + ((ch & 3) * 8 + row) * 16) = v;
    }
    const char* kfp = ws + OFF_KF + ((size_t)((h * 4 + wave) * 2) * 64 + lane) * 16;
    const char* vfp = ws + OFF_VF + ((size_t)((h * 4 + wave) * 2) * 64 + lane) * 16;
    const unsigned kmo = (unsigned)(OFF_KM + ((size_t)(h * 4 + wave) * 64 + lane) * 16);
    const unsigned vmo = (unsigned)(OFF_VM + ((size_t)((h * 4 + wave) * 2) * 64 + lane) * 8);
    const int mrd = (g * 8 + (c & 7)) * 16;
    const int srd = (c >> 3) * 1024 + (g * 16 + (c & 7)) * 16;
    const int srd2 = 2048 + ((g & 1) * 16 + (c & 7)) * 16 + (2 * (g >> 1) + (c >> 3)) * 4;
    const unsigned poff = c < 8 ? (unsigned)((((part * (32 / CPH) + wave * TPW) * 4 + (g >> 1) * 2 + (c >> 2)) * 32 + h) * 128 + ((c & 3) * 2 + (g & 1)) * 16) : 0x40000000u;
    bf16x8 kf0 = *(const bf16x8*)(kfp), kf1 = *(const bf16x8*)(kfp + 1024);
    bf16x8 vf0 = *(const bf16x8*)(vfp), vf1 = *(const bf16x8*)(vfp + 1024);
    __syncthreads();
    unsigned cap = SPIN_CAP;
#define VSTEP(ks) do { MFMA_VV(av0, mf[(ks) & 7], wv[0][ks]); MFMA_VV(av1, mf[(ks) & 7], wv[1][ks]); \
                       if ((ks) < 8) mf[(ks) & 7] = *(const bf16x8*)(l_mem + ((ks) + 8) * 512 + mrd); } while (0)
    for (int s = 0; s < NSEG_; ++s) {
        STAMP(0);
        f32x4 aq0, aq1, ak0, ak1, av0, av1;
        bf16x8 mf[8];
#pragma unroll
        for (int ks = 0; ks < 8; ++ks) mf[ks] = *(const bf16x8*)(l_mem + ks * 512 + mrd);
#pragma unroll
        for (int ks = 0; ks < 16; ++ks) {
            const bf16x8 m = mf[ks & 7];
            if (ks == 0) { MFMA_AA0(aq0, wq[0][ks], m); MFMA_AA0(aq1, wq[1][ks], m); MFMA_AA0(ak0, wk[0][ks], m); MFMA_AA0(ak1, wk[1][ks], m); }
            else { MFMA_AA(aq0, wq[0][ks], m); MFMA_AA(aq1, wq[1][ks], m); MFMA_AA(ak0, wk[0][ks], m); MFMA_AA(ak1, wk[1][ks], m); }
            mf[ks & 7] = *(const bf16x8*)(l_mem + ((ks + 8) & 15) * 512 + mrd);
        }
        MFMA_VV0(av0, mf[0], wv[0][0]); MFMA_VV0(av1, mf[0], wv[1][0]);
        mf[0] = *(const bf16x8*)(l_mem + 8 * 512 + mrd);
        asm volatile("" : "+v"(aq0), "+v"(aq1), "+v"(ak0), "+v"(ak1));
        bf16x8 qB = pack8(aq0, aq1);
        bf16x8 kA = pack8(ak0, ak1);
        NOP_IN2(qB, kA);
        f32x4 s0, s1, s2;
        MFMA_VV0(s0, kf0, qB); MFMA_VV0(s1, kf1, qB); MFMA_VV0(s2, kA, qB);
        VSTEP(1);
        asm volatile("" : "+v"(s0), "+v"(s1), "+v"(s2));
        *(f32x4*)(l_sx + (wave * 3 + 0) * 1024 + lane * 16) = s0;
        *(f32x4*)(l_sx + (wave * 3 + 1) * 1024 + lane * 16) = s1;
        *(f32x4*)(l_sx + (wave * 3 + 2) * 1024 + lane * 16) = s2;
        VSTEP(2);
        STAMP(1);
        __syncthreads();
        STAMP(2);
        f32x4 sa = *(const f32x4*)(l_sx + 0 * 3072 + srd), sb = *(const f32x4*)(l_sx + 1 * 3072 + srd);
        f32x4 sc = *(const f32x4*)(l_sx + 2 * 3072 + srd), sd = *(const f32x4*)(l_sx + 3 * 3072 + srd);
        float ma = *(const float*)(l_sx + 0 * 3072 + srd2), mb = *(const float*)(l_sx + 1 * 3072 + srd2);
        float mc = *(const float*)(l_sx + 2 * 3072 + srd2), md = *(const float*)(l_sx + 3 * 3072 + srd2);
        VSTEP(3); VSTEP(4); VSTEP(5); VSTEP(6);
        __builtin_amdgcn_sched_barrier(0);
        f32x4 e = (sa + sb) + (sc + sd);
        float em = (ma + mb) + (mc + md);
        VSTEP(7);
        __builtin_amdgcn_sched_barrier(0);
        e[0] = bf16r(bf16r(e[0]) * SCALE_); e[1] = bf16r(bf16r(e[1]) * SCALE_);
        VSTEP(8);
        __builtin_amdgcn_sched_barrier(0);
        e[2] = bf16r(bf16r(e[2]) * SCALE_); e[3] = bf16r(bf16r(e[3]) * SCALE_);
        VSTEP(9);
        __builtin_amdgcn_sched_barrier(0);
        em = bf16r(bf16r(em) * SCALE_);
        float mx = fmaxf(fmaxf(fmaxf(e[0], e[1]), fmaxf(e[2], e[3])), em);
        mx = max_x8(mx);
        VSTEP(10);
        __builtin_amdgcn_sched_barrier(0);
        mx = max_x32(max_x16(mx));
        VSTEP(11);
        __builtin_amdgcn_sched_barrier(0);
        e[0] = __builtin_amdgcn_exp2f((e[0] - mx) * LOG2E_); e[1] = __builtin_amdgcn_exp2f((e[1] - mx) * LOG2E_);
        VSTEP(12);
        __builtin_amdgcn_sched_barrier(0);
        e[2] = __builtin_amdgcn_exp2f((e[2] - mx) * LOG2E_); e[3] = __builtin_amdgcn_exp2f((e[3] - mx) * LOG2E_);
        VSTEP(13);
        __builtin_amdgcn_sched_barrier(0);
        em = __builtin_amdgcn_exp2f((em - mx) * LOG2E_);
        float sum = add_x8(((e[0] + e[1]) + (e[2] + e[3])) + em);
        VSTEP(14);
        __builtin_amdgcn_sched_barrier(0);
        sum = add_x32(add_x16(sum));
        VSTEP(15);
        __builtin_amdgcn_sched_barrier(0);
        const float inv = __builtin_amdgcn_rcpf(sum);
        e *= inv; em *= inv;
        const unsigned p0 = pack2(e[0], e[1]), p1 = pack2(e[2], e[3]);
        const u32x4 pb0u = {p0, p1, ror8(p0), ror8(p1)};
        const unsigned dm = pack2(em, __uint_as_float(ror8(__float_as_uint(em))));
        auto sw = __builtin_amdgcn_permlane32_swap(dm, dm, false, false);
        const u32x2 pb1u = {g < 2 ? sw[0] : 0u, g < 2 ? sw[1] : 0u};
        bf16x8 pB0 = __builtin_bit_cast(bf16x8, pb0u);
        bf16x4 pB1 = __builtin_bit_cast(bf16x4, pb1u);
        NOP_OUT2(av0, av1);
        bf16x4 vA0 = pack4(av0), vA1 = pack4(av1);
        if (s == NSEG_ - 1) {
            if (part == 0) {
                __builtin_amdgcn_raw_buffer_store_b128(__builtin_bit_cast(u32x4, kA), wrs, kmo + s * 131072, 0, 16);
                __builtin_amdgcn_raw_buffer_store_b64(__builtin_bit_cast(u32x2, vA0), wrs, vmo + s * 131072, 0, 16);
                __builtin_amdgcn_raw_buffer_store_b64(__builtin_bit_cast(u32x2, vA1), wrs, vmo + s * 131072 + 512, 0, 16);
                arrive(cnt_ptr(ws, NUNIT - 1, 0), tid);
            }
            break;
        }
        bf16x8 pB1x = ext8(pB1), vA0x = ext8(vA0), vA1x = ext8(vA1);
        asm volatile("s_nop 1" : "+v"(pB0), "+v"(pB1x), "+v"(vA0x), "+v"(vA1x));
        f32x4 o0, o1;
        MFMA_VV0(o0, vf0, pB0); MFMA_VV0(o1, vf1, pB0);
        MFMA_VV(o0, vA0x, pB1x); MFMA_VV(o1, vA1x, pB1x);
        NOP_OUT2(o0, o1);
        *(bf16x8*)(l_ox + wave * 1024 + lane * 16) = pack8(o0, o1);
        const char* wop = l_wo + wave * TPW * 4 * 1024 + lane * 16;
        constexpr int NM = TPW * 4;
        bf16x8 wf[8];
#pragma unroll
        for (int i = 0; i < 8; ++i) wf[i] = *(const bf16x8*)(wop + ((i % TPW) * 4 + (i / TPW)) * 1024);
        __syncthreads();
        STAMP(3);
        bf16x8 oB[4];
#pragma unroll
        for (int kk = 0; kk < 4; ++kk) oB[kk] = *(const bf16x8*)(l_ox + kk * 1024 + lane * 16);
        const unsigned tag = ((s >> 1) + 1) & 1;
        const unsigned pbase = (s & 1) * 128 * 4096 + poff;
        f32x4 pa[TPW];
#pragma unroll
        for (int i = 0; i < NM; ++i) {
            const int kk = i / TPW, t = i % TPW;
            const bf16x8 w = wf[i & 7];
            if (kk == 0) MFMA_VV0(pa[t], w, oB[kk]); else MFMA_VV(pa[t], w, oB[kk]);
            if (i + 8 < NM) { const int n = i + 8; wf[i & 7] = *(const bf16x8*)(wop + ((n % TPW) * 4 + (n / TPW)) * 1024); }
        }
        NOP_OUT2(pa[TPW - 2], pa[TPW - 1]);
#pragma unroll
        for (int q = 0; q < TPW; ++q) {
            asm volatile("" : "+v"(pa[q]));
            u32x4 u = __builtin_bit_cast(u32x4, pa[q]);
            u = (u & ~1u) | tag;
            __builtin_amdgcn_raw_buffer_store_b128(u, prs, pbase + q * 4 * 4096, 0, 16);
        }
        if (part == 0) {
            __builtin_amdgcn_raw_buffer_store_b128(__builtin_bit_cast(u32x4, kA), wrs, kmo + s * 131072, 0, 16);
            __builtin_amdgcn_raw_buffer_store_b64(__builtin_bit_cast(u32x2, vA0), wrs, vmo + s * 131072, 0, 16);
            __builtin_amdgcn_raw_buffer_store_b64(__builtin_bit_cast(u32x2, vA1), wrs, vmo + s * 131072 + 512, 0, 16);
        }
        {
            const size_t so = (size_t)(s + 1) * 32 * 8 * 1024;
            kf0 = *(const bf16x8*)(kfp + so); kf1 = *(const bf16x8*)(kfp + so + 1024);
            vf0 = *(const bf16x8*)(vfp + so); vf1 = *(const bf16x8*)(vfp + so + 1024);
        }
        STAMP(4);
        {
            unsigned off = (s + 1) * 65536 + (blockIdx.x % NCOPY) * 8192 + wave * 2048 + lane * 16;
            u32x4 y0, y1;
            for (unsigned spins = 0;; ++spins) {
                asm volatile("" : "+v"(off)::"memory");
                y0 = __builtin_amdgcn_raw_buffer_load_b128(mrs, off, 0, 16);
                y1 = __builtin_amdgcn_raw_buffer_load_b128(mrs, off + 1024, 0, 16);
                const bool ok = (y0[0] != 0xFFFFFFFFu) & (y0[1] != 0xFFFFFFFFu) & (y0[2] != 0xFFFFFFFFu) & (y0[3] != 0xFFFFFFFFu) &
                                (y1[0] != 0xFFFFFFFFu) & (y1[1] != 0xFFFFFFFFu) & (y1[2] != 0xFFFFFFFFu) & (y1[3] != 0xFFFFFFFFu);
                if (__all(ok)) break;
                if (spins >= cap) { cap = 0; break; }
            }
            STAMP(5);
            *(u32x4*)(l_mem + wave * 2048 + lane * 16) = y0;
            *(u32x4*)(l_mem + wave * 2048 + 1024 + lane * 16) = y1;
        }
        asm volatile("s_waitcnt vmcnt(0)" ::: "memory");
        __syncthreads();
        if (part == 0 && tid == 0) {
            const int u = unit_of_last_seg(s);
            if (u >= 0) __hip_atomic_fetch_add(cnt_ptr(ws, u, 0), 1u, __ATOMIC_RELAXED, __HIP_MEMORY_SCOPE_AGENT);
        }
    }
}

extern "C" void kernel_launch(void* const* d_in, const int* in_sizes, int n_in, void* d_out, int out_size, void* d_ws,
                              size_t ws_size, hipStream_t stream) {
    const unsigned short* x = (const unsigned short*)d_in[0];
    const unsigned short* mem0 = (const unsigned short*)d_in[1];
    const unsigned short* Wq = (const unsigned short*)d_in[2];
    const unsigned short* Wk = (const unsigned short*)d_in[3];
    const unsigned short* Wv = (const unsigned short*)d_in[4];
    const unsigned short* Wo = (const unsigned short*)d_in[5];
    const unsigned short* Wg = (const unsigned short*)d_in[8];
    const unsigned short* Wu = (const unsigned short*)d_in[9];
    const unsigned short* Wd = (const unsigned short*)d_in[10];
    char* ws = (char*)d_ws;
    prep_kernel<<<NB_PREP, 256, 0, stream>>>(x, Wq, Wk, Wv, Wo, Wg, Wu, Wd, ws);
    kv_kernel<<<256, 256, 0, stream>>>(ws);
    chain_kernel<<<NCW + NRW + NWK, 256, 0, stream>>>(mem0, x, (unsigned short*)d_out, ws);
}
```

```cpp
#include <hip/hip_runtime.h>
#include <hip/hip_bf16.h>
#include <stdint.h>
#include <type_traits>

constexpr int S_ = 2048, D_ = 512, H_ = 32, DH_ = 128, M_ = 8, SEG_ = 32, NSEG_ = 64, F_ = 1792;

typedef short bf16x8 __attribute__((ext_vector_type(8)));
typedef short bf16x4 __attribute__((ext_vector_type(4)));
typedef float f32x4 __attribute__((ext_vector_type(4)));
typedef unsigned u32x4 __attribute__((ext_vector_type(4)));
typedef unsigned u32x2 __attribute__((ext_vector_type(2)));
typedef __bf16 bfv2 __attribute__((ext_vector_type(2)));

__device__ __forceinline__ unsigned pack2(float a, float b) {
    bfv2 v = {(__bf16)a, (__bf16)b};
    return __builtin_bit_cast(unsigned, v);
}
__device__ __forceinline__ float bf16r(float a) { return (float)(__bf16)a; }
__device__ __forceinline__ float bflo(unsigned u) { return __uint_as_float(u << 16); }
__device__ __forceinline__ float bfhi(unsigned u) { return __uint_as_float(u & 0xffff0000u); }
__device__ __forceinline__ bf16x8 pack8(f32x4 a, f32x4 b) {
    u32x4 r = {pack2(a[0], a[1]), pack2(a[2], a[3]), pack2(b[0], b[1]), pack2(b[2], b[3])};
    return __builtin_bit_cast(bf16x8, r);
}
__device__ __forceinline__ bf16x4 pack4(f32x4 a) {
    u32x2 r = {pack2(a[0], a[1]), pack2(a[2], a[3])};
    return __builtin_bit_cast(bf16x4, r);
}
__device__ __forceinline__ int get_lane() {
    int l;
    asm volatile("v_mbcnt_lo_u32_b32 %0, -1, 0\n\tv_mbcnt_hi_u32_b32 %0, -1, %0" : "=v"(l));
    return l;
}
#define MFMA32(a, b, c) __builtin_amdgcn_mfma_f32_16x16x32_bf16(a, b, c, 0, 0, 0)
__device__ __forceinline__ bf16x8 ext8(bf16x4 a) { bf16x8 r = {a[0], a[1], a[2], a[3], 0, 0, 0, 0}; return r; }
#define MFMA16(a, b, c) MFMA32(ext8(a), ext8(b), c)

constexpr size_t MB = 1024 * 1024;
constexpr size_t OFF_WQKV = 0;
constexpr size_t OFF_WO = OFF_WQKV + 12 * MB;
constexpr size_t OFF_WGU = OFF_WO + 4 * MB;
constexpr size_t OFF_WD = OFF_WGU + 4 * MB;
constexpr size_t OFF_XN = OFF_WD + 2 * MB;
constexpr size_t OFF_QF = OFF_XN + 2 * MB;
constexpr size_t OFF_KF = OFF_QF + 16 * MB;
constexpr size_t OFF_VF = OFF_KF + 16 * MB;
constexpr size_t OFF_KM = OFF_VF + 16 * MB;
constexpr size_t OFF_VM = OFF_KM + 8 * MB;
constexpr size_t OFF_OF = OFF_VM + 8 * MB;
constexpr size_t OFF_P1 = OFF_OF + 16 * MB;
constexpr size_t OFF_H = OFF_P1 + 32 * MB;
constexpr size_t OFF_HN = OFF_H + 2 * MB;
constexpr size_t OFF_AF = OFF_HN + 2 * MB;
constexpr size_t OFF_P2 = OFF_AF + 8 * MB;
constexpr size_t OFF_PART = OFF_P2 + 16 * MB;
constexpr size_t OFF_MEMV = OFF_PART + 1 * MB;
constexpr size_t OFF_CNT = OFF_MEMV + 4 * MB;
constexpr size_t WS_END = OFF_CNT + 1 * MB;

__device__ __forceinline__ float wave_sum(float v) {
#pragma unroll
    for (int m = 1; m < 64; m <<= 1) v += __shfl_xor(v, m);
    return v;
}
__device__ __forceinline__ u32x4 ln_row(const float (&x)[8]) {
    float s = 0.f;
#pragma unroll
    for (int j = 0; j < 8; ++j) s += x[j];
    const float mean = wave_sum(s) * (1.0f / 512.0f);
    float q = 0.f;
#pragma unroll
    for (int j = 0; j < 8; ++j) { const float d = x[j] - mean; q += d * d; }
    const float var = wave_sum(q) * (1.0f / 512.0f);
    const float mb = bf16r(mean);
    const float vb = bf16r(var);
    const float den = bf16r(sqrtf(bf16r(vb + 1e-5f)));
    float y[8];
#pragma unroll
    for (int j = 0; j < 8; ++j) y[j] = bf16r(x[j] - mb) / den;
    u32x4 r = {pack2(y[0], y[1]), pack2(y[2], y[3]), pack2(y[4], y[5]), pack2(y[6], y[7])};
    return r;
}
__device__ __forceinline__ void store_row_frag(char* base, int row, int lane, u32x4 v) {
    const int tt = row >> 4, c = row & 15, ks = lane >> 2, g = lane & 3;
    *(u32x4*)(base + ((size_t)(tt * 16 + ks) * 64 + g * 16 + c) * 16) = v;
}

constexpr int NB_QKV = 3 * 256, NB_WO = 256, NB_GU = 2 * 112, NB_WD = 112, NB_LN = 512, NB_CLR = 64 + 256 + 4;
constexpr int NB_PREP = NB_QKV + NB_WO + NB_GU + NB_WD + NB_LN + NB_CLR;

__global__ void __launch_bounds__(256) prep_kernel(const unsigned short* __restrict__ x, const unsigned short* __restrict__ Wq,
                                                   const unsigned short* __restrict__ Wk, const unsigned short* __restrict__ Wv,
                                                   const unsigned short* __restrict__ Wo, const unsigned short* __restrict__ Wg,
                                                   const unsigned short* __restrict__ Wu, const unsigned short* __restrict__ Wd,
                                                   char* __restrict__ ws) {
    __shared__ __attribute__((aligned(16))) unsigned short t[32][264];
    int b = blockIdx.x;
    const int tid = threadIdx.x, wave = tid >> 6, lane = tid & 63;
    const unsigned short* src; int N, ks, nb, KS; bool perm = false; char* dst; int mode = 0;
    if (b < NB_QKV) {
        const int m = b >> 8; b &= 255;
        src = m == 0 ? Wq : (m == 1 ? Wk : Wv); N = 4096; KS = 16; ks = b & 15; nb = b >> 4; dst = ws + OFF_WQKV + (size_t)m * 4 * MB;
    } else if (b < NB_QKV + NB_WO) {
        b -= NB_QKV; src = Wo; N = 512; KS = 128; ks = b & 127; nb = b >> 7; perm = true; dst = ws + OFF_WO;
    } else if (b < NB_QKV + NB_WO + NB_GU) {
        b -= NB_QKV + NB_WO; const int up = b >= 112; if (up) b -= 112;
        src = up ? Wu : Wg; N = 1792; KS = 16; ks = b & 15; nb = b >> 4; dst = ws + OFF_WGU; mode = 1 + up;
    } else if (b < NB_QKV + NB_WO + NB_GU + NB_WD) {
        b -= NB_QKV + NB_WO + NB_GU; src = Wd; N = 512; KS = 56; ks = b % 56; nb = b / 56; perm = true; dst = ws + OFF_WD;
    } else if (b < NB_QKV + NB_WO + NB_GU + NB_WD + NB_LN) {
        b -= NB_QKV + NB_WO + NB_GU + NB_WD;
        const int row = b * 4 + wave;
        const u32x4 v = *(const u32x4*)(x + (size_t)row * 512 + lane * 8);
        float f[8] = {bflo(v[0]), bfhi(v[0]), bflo(v[1]), bfhi(v[1]), bflo(v[2]), bfhi(v[2]), bflo(v[3]), bfhi(v[3])};
        store_row_frag(ws + OFF_XN, row, lane, ln_row(f));
        return;
    } else {
        b -= NB_QKV + NB_WO + NB_GU + NB_WD + NB_LN;
        const bool p = b < 64 || b >= 320;
        char* d = b < 64 ? ws + OFF_PART + (size_t)b * 16384 : ws + OFF_MEMV + (size_t)(b - 64) * 16384;
        const unsigned f = p ? 0u : 0xFFFFFFFFu;
        const u32x4 v = {f, f, f, f};
#pragma unroll
        for (int i = 0; i < 4; ++i) *(u32x4*)(d + i * 4096 + tid * 16) = v;
        return;
    }
    {
        u32x4 v[4];
#pragma unroll
        for (int i = 0; i < 4; ++i) v[i] = *(const u32x4*)(src + (size_t)(ks * 32 + i * 8 + (tid >> 5)) * N + nb * 256 + (tid & 31) * 8);
#pragma unroll
        for (int i = 0; i < 4; ++i) *(u32x4*)&t[i * 8 + (tid >> 5)][(tid & 31) * 8] = v[i];
    }
    __syncthreads();
    const int c = lane & 15, g = lane >> 4;
#pragma unroll
    for (int q = 0; q < 4; ++q) {
        unsigned short e[8];
#pragma unroll
        for (int j = 0; j < 8; ++j) {
            const int k = perm ? (j < 4 ? 4 * g + j : 16 + 4 * g + j - 4) : 8 * g + j;
            e[j] = t[k][(q * 4 + wave) * 16 + c];
        }
        int nt = nb * 16 + q * 4 + wave;
        if (mode) nt = (nt >> 1) * 4 + (mode == 2 ? 2 : 0) + (nt & 1);
        u32x4 o = {e[0] | ((unsigned)e[1] << 16), e[2] | ((unsigned)e[3] << 16), e[4] | ((unsigned)e[5] << 16), e[6] | ((unsigned)e[7] << 16)};
        *(u32x4*)(dst + ((size_t)(nt * KS + ks) * 64 + lane) * 16) = o;
    }
}

enum { G_KV = 0, G_OUTP = 1, G_FFN1 = 2, G_DOWN = 3, G_Q = 4 };

__device__ __forceinline__ void st16_dev(const __amdgpu_buffer_rsrc_t rs, size_t off, u32x4 v) {
    __builtin_amdgcn_raw_buffer_store_b128(v, rs, (unsigned)off, 0, 16);
}

template <int MODE, int TT, class SYNC>
__device__ __forceinline__ void gemm_core(char* ws, char* xl, const __amdgpu_buffer_rsrc_t wrs, int tt0, int cb, int wave, SYNC sync) {
    int lane;
    constexpr int NKS = MODE == G_DOWN ? 14 : 16;
    constexpr int KST = MODE == G_OUTP ? 128 : (MODE == G_DOWN ? 56 : 16);
    constexpr int NT = MODE == G_FFN1 ? 4 : 2;
    constexpr int NIT = 1;
    lane = get_lane();
    int ks0 = 0, cg = cb;
    const char* xsrc; unsigned wsrc;
    if (MODE == G_KV || MODE == G_Q) { xsrc = ws + OFF_XN; wsrc = (unsigned)OFF_WQKV; }
    else if (MODE == G_OUTP) { xsrc = ws + OFF_OF; wsrc = (unsigned)OFF_WO; ks0 = (cb & 7) * 16; cg = cb >> 3; }
    else if (MODE == G_FFN1) { xsrc = ws + OFF_HN; wsrc = (unsigned)OFF_WGU; }
    else { xsrc = ws + OFF_AF; wsrc = (unsigned)OFF_WD; ks0 = (cb & 3) * 14; cg = cb >> 2; }
    const int c = lane & 15, g = lane >> 4;
    auto tile0 = [&](int it) -> int {
        if (MODE == G_KV) { const int ci = 32 + cg; return (ci >> 5) * 256 + (ci & 31) * 8 + wave * 2; }
        else if (MODE == G_Q) return cg * 8 + wave * 2;
        else if (MODE == G_FFN1) return (cg * 4 + wave) * 4;
        else return cg * 8 + wave * 2;
    };
    constexpr int PD = NKS / 2;
    bf16x8 wr[PD][NT];
    const unsigned wvo = wsrc + (unsigned)((tile0(0) * KST + ks0) * 64 + lane) * 16;
#define LD_W(t, ks) __builtin_bit_cast(bf16x8, __builtin_amdgcn_raw_buffer_load_b128(wrs, wvo, ((t) * KST + (ks)) * 1024, 0))
#pragma unroll
    for (int ks = 0; ks < PD; ++ks)
#pragma unroll
        for (int t = 0; t < NT; ++t) wr[ks][t] = LD_W(t, ks);
    sync();
#pragma unroll
    for (int j = 0; j < (TT * NKS + 3) / 4; ++j) {
        const int i = j * 4 + wave;
        if (i < TT * NKS) {
            const int tt = i / NKS, ks = i % NKS;
            if constexpr (MODE == G_KV || MODE == G_Q)
                __builtin_amdgcn_global_load_lds((const unsigned*)(xsrc + ((size_t)((tt0 + tt) * KST + ks0 + ks) * 64 + lane) * 16),
                                                 (unsigned*)(xl + (size_t)i * 1024 + lane * 16), 16, 0, 0);
            else
                __builtin_amdgcn_global_load_lds((const unsigned*)(xsrc + ((size_t)((tt0 + tt) * KST + ks0 + ks) * 64 + lane) * 16),
                                                 (unsigned*)(xl + (size_t)i * 1024 + lane * 16), 16, 0, 16);
        }
    }
    asm volatile("s_waitcnt vmcnt(0)" ::: "memory");
    __syncthreads();
#pragma unroll 1
    for (int it = 0; it < NIT; ++it) {
        asm volatile("" ::: "memory");
        const int nt0 = tile0(it);
        int mat = 0, head = 0;
        if (MODE == G_KV) { const int ci = 32 + cg; mat = ci >> 5; head = ci & 31; }
        f32x4 acc[NT][TT];
#pragma unroll
        for (int t = 0; t < NT; ++t)
#pragma unroll
            for (int tt = 0; tt < TT; ++tt) acc[t][tt] = (f32x4){0.f, 0.f, 0.f, 0.f};
        const bool vmat = (MODE == G_KV) && (mat == 2);
        auto kloop = [&](auto swapped) {
            bf16x8 xf[2][TT];
#pragma unroll
            for (int tt = 0; tt < TT; ++tt) xf[0][tt] = *(const bf16x8*)(xl + (tt * NKS) * 1024 + lane * 16);
#pragma unroll
            for (int ks = 0; ks < NKS; ++ks) {
                bf16x8 wf[NT];
#pragma unroll
                for (int t = 0; t < NT; ++t) {
                    wf[t] = wr[ks % PD][t];
                    if (ks + PD < NKS) wr[ks % PD][t] = LD_W(t, ks + PD);
                }
                if (ks + 1 < NKS) {
#pragma unroll
                    for (int tt = 0; tt < TT; ++tt) xf[(ks + 1) & 1][tt] = *(const bf16x8*)(xl + (tt * NKS + ks + 1) * 1024 + lane * 16);
                }
                __builtin_amdgcn_sched_barrier(0);
#pragma unroll
                for (int tt = 0; tt < TT; ++tt)
#pragma unroll
                    for (int t = 0; t < NT; ++t) {
                        if constexpr (decltype(swapped)::value) acc[t][tt] = MFMA32(xf[ks & 1][tt], wf[t], acc[t][tt]);
                        else acc[t][tt] = MFMA32(wf[t], xf[ks & 1][tt], acc[t][tt]);
                    }
                __builtin_amdgcn_sched_barrier(0);
            }
        };
        if (vmat) kloop(std::true_type{}); else kloop(std::false_type{});
        if (MODE == G_KV) {
            if (mat == 2) {
#pragma unroll
                for (int sg = 0; sg < TT / 2; ++sg)
#pragma unroll
                    for (int t = 0; t < 2; ++t) {
                        const int seg = tt0 / 2 + sg;
                        st16_dev(wrs, OFF_VF + ((size_t)(((seg * 32 + head) * 4 + wave) * 2 + t) * 64 + lane) * 16, __builtin_bit_cast(u32x4, pack8(acc[t][2 * sg], acc[t][2 * sg + 1])));
                    }
            } else {
#pragma unroll
                for (int tt = 0; tt < TT; ++tt) {
                    const int seg = tt0 / 2 + (tt >> 1);
                    st16_dev(wrs, OFF_KF + ((size_t)(((seg * 32 + head) * 4 + wave) * 2 + (tt & 1)) * 64 + lane) * 16, __builtin_bit_cast(u32x4, pack8(acc[0][tt], acc[1][tt])));
                }
            }
        } else if (MODE == G_Q) {
            __syncthreads();
#pragma unroll
            for (int tt = 0; tt < TT; ++tt) *(bf16x8*)(xl + (tt * 4 + wave) * 1024 + lane * 16) = pack8(acc[0][tt], acc[1][tt]);
        } else if (MODE == G_OUTP || MODE == G_DOWN) {
            const size_t dst = (MODE == G_OUTP ? OFF_P1 : OFF_P2) + (size_t)(MODE == G_OUTP ? (cb & 7) : (cb & 3)) * S_ * D_ * 4;
#pragma unroll
            for (int t = 0; t < 2; ++t)
#pragma unroll
                for (int tt = 0; tt < TT; ++tt)
                    st16_dev(wrs, dst + ((size_t)((tt0 + tt) * 16 + c) * D_ + (nt0 + t) * 16 + g * 4) * 4, __builtin_bit_cast(u32x4, acc[t][tt]));
        } else {
            const int ag = cg * 4 + wave;
#pragma unroll
            for (int tt = 0; tt < TT; ++tt) {
                f32x4 a0, a1;
#pragma unroll
                for (int i = 0; i < 4; ++i) {
                    a0[i] = bf16r(acc[0][tt][i]) * bf16r(acc[2][tt][i]);
                    a1[i] = bf16r(acc[1][tt][i]) * bf16r(acc[NT - 1][tt][i]);
                }
                st16_dev(wrs, OFF_AF + ((size_t)((tt0 + tt) * 56 + ag) * 64 + lane) * 16, __builtin_bit_cast(u32x4, pack8(a0, a1)));
            }
        }
    }
}

constexpr float SCALE_ = 0.08838834764831845f;
constexpr float LOG2E_ = 1.4426950408889634f;
constexpr unsigned SPIN_CAP = 1u << 17;

#define FS(i) do { } while (0)
#define STAMP(i) do { } while (0)
__device__ __forceinline__ float max_x16(float v) {
    auto r = __builtin_amdgcn_permlane16_swap(__float_as_uint(v), __float_as_uint(v), false, false);
    return fmaxf(__uint_as_float(r[0]), __uint_as_float(r[1]));
}
__device__ __forceinline__ float max_x32(float v) {
    auto r = __builtin_amdgcn_permlane32_swap(__float_as_uint(v), __float_as_uint(v), false, false);
    return fmaxf(__uint_as_float(r[0]), __uint_as_float(r[1]));
}
__device__ __forceinline__ float add_x16(float v) {
    auto r = __builtin_amdgcn_permlane16_swap(__float_as_uint(v), __float_as_uint(v), false, false);
    return __uint_as_float(r[0]) + __uint_as_float(r[1]);
}
__device__ __forceinline__ float add_x32(float v) {
    auto r = __builtin_amdgcn_permlane32_swap(__float_as_uint(v), __float_as_uint(v), false, false);
    return __uint_as_float(r[0]) + __uint_as_float(r[1]);
}
__device__ __forceinline__ float add_x8(float v) {
    return v + __uint_as_float(__builtin_amdgcn_update_dpp(0u, __float_as_uint(v), 0x128, 0xf, 0xf, false));
}
#define MFMA_AA(acc, wa, b) asm volatile("v_mfma_f32_16x16x32_bf16 %0, %1, %2, %0" : "+v"(acc) : "a"(wa), "v"(b))
#define MFMA_AA0(acc, wa, b) asm volatile("v_mfma_f32_16x16x32_bf16 %0, %1, %2, 0" : "=&v"(acc) : "a"(wa), "v"(b))
#define MFMA_VV(acc, a, b) asm volatile("v_mfma_f32_16x16x32_bf16 %0, %1, %2, %0" : "+v"(acc) : "v"(a), "v"(b))
#define MFMA_VV0(acc, a, b) asm volatile("v_mfma_f32_16x16x32_bf16 %0, %1, %2, 0" : "=&v"(acc) : "v"(a), "v"(b))
#define NOP_IN2(a, b) asm volatile("s_nop 1" : "+v"(a), "+v"(b))
#define NOP_OUT2(a, b) asm volatile("s_nop 7\n\ts_nop 3" : "+v"(a), "+v"(b))
#define NOP_OUT4(a, b, c, d) asm volatile("s_nop 7\n\ts_nop 3" : "+v"(a), "+v"(b), "+v"(c), "+v"(d))

#define CPH 2
constexpr int NCW = 32 * CPH;
constexpr int TPW = 8 / CPH;
__device__ __forceinline__ float max_x8(float v) {
    return fmaxf(v, __uint_as_float(__builtin_amdgcn_update_dpp(0u, __float_as_uint(v), 0x128, 0xf, 0xf, false)));
}
__device__ __forceinline__ unsigned ror8(unsigned v) { return __builtin_amdgcn_update_dpp(0u, v, 0x128, 0xf, 0xf, false); }

#define HINTS 0
#define HPOS 2
#define DROPL 0
#define GDELAY 0
#define H2POS 1
#define HDELAY 0
#define NCOPY 8
#define GDEC 1
#define GINC 15
#define GMIN 60
#define SLBIG 32
#define SLCH 127
#define RWV 2
constexpr int NRW = 128 / RWV;
constexpr int NWK = 128;
constexpr int NUNIT = 21;
__device__ __forceinline__ int unit_of_last_seg(int s) {
    if (s < 52) return (s & 3) == 3 ? (s >> 2) : -1;
    if (s < 60) return (s & 1) ? 13 + ((s - 52) >> 1) : -1;
    return 17 + (s - 60);
}
constexpr unsigned HINT1 = (unsigned)(OFF_CNT + 32768), HINT2 = (unsigned)(OFF_CNT + 32768 + 16384);
__device__ __forceinline__ void wait_hint(const __amdgpu_buffer_rsrc_t rs, unsigned off, unsigned want, unsigned cap) {
    cap = (unsigned)__builtin_amdgcn_readfirstlane(cap);
    for (unsigned spins = 0; spins <= cap; ++spins) {
        asm volatile("" : "+v"(off)::"memory");
        const unsigned v = __builtin_amdgcn_raw_buffer_load_b32(rs, off, 0, 16);
        if ((unsigned)__builtin_amdgcn_readfirstlane(v) >= want) break;
    }
}
typedef __attribute__((address_space(1))) unsigned gu32;
__device__ __forceinline__ gu32* cnt_ptr(char* ws, int u, int ph) { return (gu32*)(ws + OFF_CNT + (size_t)(u * 8 + ph) * 128); }
__device__ __forceinline__ void arrive(gu32* c, int wave) {
    asm volatile("s_waitcnt vmcnt(0)" ::: "memory");
    __syncthreads();
    if (wave == 0 && get_lane() == 0) __hip_atomic_fetch_add(c, 1u, __ATOMIC_RELAXED, __HIP_MEMORY_SCOPE_AGENT);
}
template <int SLP>
__device__ __forceinline__ void wait_for(gu32* c, unsigned target, int wave, unsigned& cap) {
    if (wave == 0) {
        for (unsigned spins = 0;; ++spins) {
            if ((unsigned)__builtin_amdgcn_readfirstlane(__hip_atomic_load(c, __ATOMIC_RELAXED, __HIP_MEMORY_SCOPE_AGENT)) >= target) break;
            if (spins >= cap) { cap = 0; break; }
            __builtin_amdgcn_s_sleep(SLP);
        }
    }
    asm volatile("" ::: "memory");
    __syncthreads();
}

#define LD16_DEV(off) __builtin_bit_cast(bf16x8, __builtin_amdgcn_raw_buffer_load_b128(wrs, (unsigned)(off), 0, 16))
#define LD8_DEV(off) __builtin_bit_cast(bf16x4, __builtin_amdgcn_raw_buffer_load_b64(wrs, (unsigned)(off), 0, 16))
__device__ __forceinline__ void seg_attention(const char* qf, const __amdgpu_buffer_rsrc_t wrs, int seg, int h,
                                              const bf16x8 (&kf)[8], const bf16x8 (&vf)[8]) {
    const int lane = get_lane();
    const int c = lane & 15, g = lane >> 4;
    bf16x8 km[4]; bf16x4 vm[8];
#pragma unroll
    for (int w = 0; w < 4; ++w) km[w] = LD16_DEV(OFF_KM + ((size_t)((seg * 32 + h) * 4 + w) * 64 + lane) * 16);
#pragma unroll
    for (int i = 0; i < 8; ++i) vm[i] = LD8_DEV(OFF_VM + ((size_t)((seg * 32 + h) * 8 + i) * 64 + lane) * 8);
    __builtin_amdgcn_sched_barrier(0);
    const f32x4 z = {0, 0, 0, 0};
    f32x4 sc[2][3];
#pragma unroll
    for (int qt = 0; qt < 2; ++qt)
#pragma unroll
        for (int kt = 0; kt < 3; ++kt) sc[qt][kt] = z;
    bf16x8 q[2][4];
#pragma unroll
    for (int w = 0; w < 4; ++w) {
        q[0][w] = *(const bf16x8*)(qf + (0 * 4 + w) * 1024 + lane * 16);
        q[1][w] = *(const bf16x8*)(qf + (1 * 4 + w) * 1024 + lane * 16);
        sc[0][0] = MFMA32(kf[w * 2], q[0][w], sc[0][0]); sc[0][1] = MFMA32(kf[w * 2 + 1], q[0][w], sc[0][1]);
        sc[1][0] = MFMA32(kf[w * 2], q[1][w], sc[1][0]); sc[1][1] = MFMA32(kf[w * 2 + 1], q[1][w], sc[1][1]);
    }
#pragma unroll
    for (int w = 0; w < 4; ++w) { sc[0][2] = MFMA32(km[w], q[0][w], sc[0][2]); sc[1][2] = MFMA32(km[w], q[1][w], sc[1][2]); }
    bf16x8 pB0[2]; bf16x4 pB1[2];
#pragma unroll
    for (int qt = 0; qt < 2; ++qt) {
        const int qi = qt * 16 + c;
        float mx = -3.0e38f;
#pragma unroll
        for (int kt = 0; kt < 3; ++kt)
#pragma unroll
            for (int i = 0; i < 4; ++i) {
                float v = bf16r(bf16r(sc[qt][kt][i]) * SCALE_);
                const bool valid = kt == 2 ? (g < 2) : (kt * 16 + 4 * g + i <= qi);
                v = valid ? v : -3.0e38f;
                sc[qt][kt][i] = v;
                mx = fmaxf(mx, v);
            }
        mx = max_x32(max_x16(mx));
        float sum = 0.f;
#pragma unroll
        for (int kt = 0; kt < 3; ++kt)
#pragma unroll
            for (int i = 0; i < 4; ++i) {
                const bool valid = kt == 2 ? (g < 2) : (kt * 16 + 4 * g + i <= qi);
                const float e = valid ? __builtin_amdgcn_exp2f((sc[qt][kt][i] - mx) * LOG2E_) : 0.f;
                sc[qt][kt][i] = e;
                sum += e;
            }
        sum = add_x32(add_x16(sum));
        const float inv = __builtin_amdgcn_rcpf(sum);
#pragma unroll
        for (int kt = 0; kt < 3; ++kt) sc[qt][kt] *= inv;
        pB0[qt] = pack8(sc[qt][0], sc[qt][1]);
        pB1[qt] = pack4(sc[qt][2]);
    }
#pragma unroll
    for (int w = 0; w < 4; ++w) {
#pragma unroll
        for (int qt = 0; qt < 2; ++qt) {
            f32x4 o0 = MFMA32(vf[w * 2], pB0[qt], z);
            f32x4 o1 = MFMA32(vf[w * 2 + 1], pB0[qt], z);
            o0 = MFMA16(vm[w * 2], pB1[qt], o0);
            o1 = MFMA16(vm[w * 2 + 1], pB1[qt], o1);
            st16_dev(wrs, OFF_OF + ((size_t)((seg * 2 + qt) * 128 + h * 4 + w) * 64 + lane) * 16, __builtin_bit_cast(u32x4, pack8(o0, o1)));
        }
    }
}

#define WSTAMP(i) do { } while (0)
template <int TT>
__device__ __forceinline__ void worker_unit(const unsigned short* x, unsigned short* out, char* ws, char* lds, const __amdgpu_buffer_rsrc_t wrs,
                                            int u, int seg0, int wg, int wave, unsigned& cap) {
    constexpr int SL = TT > 4 ? SLBIG : 2;
    const int tt0 = seg0 * 2;
    WSTAMP(0);
    gemm_core<G_Q, TT>(ws, lds, wrs, tt0, wg, wave, [] {});
    WSTAMP(1);
    {
        wait_for<16>(cnt_ptr(ws, seg0 >> 2, 6), 64, wave, cap);
        bf16x8 kf[8], vf[8];
        const int sw = wave < TT / 2 ? wave : 0;
        {
            const int lane = get_lane();
            const unsigned fb = (unsigned)(OFF_KF + ((size_t)((seg0 + sw) * 32 + wg) * 8 * 64 + lane) * 16);
#pragma unroll
            for (int i = 0; i < 8; ++i) { kf[i] = LD16_DEV(fb + i * 1024); vf[i] = LD16_DEV(fb + 16 * MB + i * 1024); }
        }
        wait_for<(TT > 4 ? SLCH : (TT > 2 ? 32 : 2))>(cnt_ptr(ws, u, 0), 32, wave, cap);
        WSTAMP(2);
        if (wave < TT / 2) seg_attention(lds + wave * 8192, wrs, seg0 + wave, wg, kf, vf);
    }
    WSTAMP(3);
    arrive(cnt_ptr(ws, u, 1), wave);
    gemm_core<G_OUTP, TT>(ws, lds, wrs, tt0, wg, wave, [&] { wait_for<SL>(cnt_ptr(ws, u, 1), 32, wave, cap); WSTAMP(4); });
    WSTAMP(5);
    arrive(cnt_ptr(ws, u, 2), wave);
    wait_for<SL>(cnt_ptr(ws, u, 2), 32, wave, cap);
    WSTAMP(6);
    const int lrow = wg * 4 + wave;
    const int row = tt0 * 16 + lrow;
    if (lrow < TT * 16) {
        const int lane = get_lane();
        const unsigned po = (unsigned)(OFF_P1 + ((size_t)row * 512 + lane * 8) * 4);
        f32x4 pv[16];
#pragma unroll
        for (int k = 0; k < 8; ++k) {
            pv[2 * k] = __builtin_bit_cast(f32x4, __builtin_amdgcn_raw_buffer_load_b128(wrs, po, k * S_ * D_ * 4, 16));
            pv[2 * k + 1] = __builtin_bit_cast(f32x4, __builtin_amdgcn_raw_buffer_load_b128(wrs, po + 16, k * S_ * D_ * 4, 16));
        }
        const u32x4 v = *(const u32x4*)(x + (size_t)row * 512 + lane * 8);
        __builtin_amdgcn_sched_barrier(0);
        f32x4 a0 = pv[0], a1 = pv[1];
#pragma unroll
        for (int k = 1; k < 8; ++k) { a0 += pv[2 * k]; a1 += pv[2 * k + 1]; }
        float f[8] = {bflo(v[0]), bfhi(v[0]), bflo(v[1]), bfhi(v[1]), bflo(v[2]), bfhi(v[2]), bflo(v[3]), bfhi(v[3])};
#pragma unroll
        for (int j = 0; j < 4; ++j) { f[j] = bf16r(f[j] + bf16r(a0[j])); f[4 + j] = bf16r(f[4 + j] + bf16r(a1[j])); }
        const u32x4 hv = {pack2(f[0], f[1]), pack2(f[2], f[3]), pack2(f[4], f[5]), pack2(f[6], f[7])};
        st16_dev(wrs, OFF_H + ((size_t)row * 512 + lane * 8) * 2, hv);
        st16_dev(wrs, OFF_HN + ((size_t)((row >> 4) * 16 + (lane >> 2)) * 64 + (lane & 3) * 16 + (row & 15)) * 16, ln_row(f));
    }
    WSTAMP(7);
    arrive(cnt_ptr(ws, u, 3), wave);
    if (wg < 28) gemm_core<G_FFN1, TT / 2>(ws, lds, wrs, tt0 + (wg / 14) * (TT / 2), wg % 14, wave, [&] { wait_for<SL>(cnt_ptr(ws, u, 3), 32, wave, cap); WSTAMP(8); });
    WSTAMP(9);
    arrive(cnt_ptr(ws, u, 4), wave);
    gemm_core<G_DOWN, TT / 2>(ws, lds, wrs, tt0 + (wg >> 4) * (TT / 2), wg & 15, wave, [&] { wait_for<SL>(cnt_ptr(ws, u, 4), 32, wave, cap); WSTAMP(10); });
    WSTAMP(11);
    arrive(cnt_ptr(ws, u, 5), wave);
    wait_for<SL>(cnt_ptr(ws, u, 5), 32, wave, cap);
    WSTAMP(12);
    if (lrow < TT * 16) {
        const int lane = get_lane();
        const size_t i = (size_t)row * 512 + lane * 8;
        const unsigned po = (unsigned)(OFF_P2 + i * 4);
        f32x4 pv[8];
#pragma unroll
        for (int k = 0; k < 4; ++k) {
            pv[2 * k] = __builtin_bit_cast(f32x4, __builtin_amdgcn_raw_buffer_load_b128(wrs, po, k * S_ * D_ * 4, 16));
            pv[2 * k + 1] = __builtin_bit_cast(f32x4, __builtin_amdgcn_raw_buffer_load_b128(wrs, po + 16, k * S_ * D_ * 4, 16));
        }
        const u32x4 v = __builtin_amdgcn_raw_buffer_load_b128(wrs, (unsigned)(OFF_H + i * 2), 0, 16);
        __builtin_amdgcn_sched_barrier(0);
        const f32x4 a0 = (pv[0] + pv[2]) + (pv[4] + pv[6]), a1 = (pv[1] + pv[3]) + (pv[5] + pv[7]);
        const u32x4 o = {pack2(bflo(v[0]) + bf16r(a0[0]), bfhi(v[0]) + bf16r(a0[1])), pack2(bflo(v[1]) + bf16r(a0[2]), bfhi(v[1]) + bf16r(a0[3])),
                         pack2(bflo(v[2]) + bf16r(a1[0]), bfhi(v[2]) + bf16r(a1[1])), pack2(bflo(v[3]) + bf16r(a1[2]), bfhi(v[3]) + bf16r(a1[3]))};
        *(u32x4*)(out + i) = o;
    }
    WSTAMP(13);
}

__global__ void __launch_bounds__(256, 1) chain_kernel(const unsigned short* mem0, const unsigned short* x, unsigned short* out, char* ws) {
    __shared__ __attribute__((aligned(16))) char lds[CPH > 1 ? 128 * 1024 : 128 * 1024 + 24576];
    char* const l_mem = lds;
    char* const l_sx = l_mem + 8192;
    char* const l_ox = l_sx + 12288;
    char* const l_wo = l_ox + 4096;
    const int wave = __builtin_amdgcn_readfirstlane(threadIdx.x >> 6);
    const int lane = get_lane();
    const int tid = wave * 64 + lane;
    const __amdgpu_buffer_rsrc_t prs = __builtin_amdgcn_make_buffer_rsrc(ws + OFF_PART, 0, 1024 * 1024, 0x00020000);
    const __amdgpu_buffer_rsrc_t mrs = __builtin_amdgcn_make_buffer_rsrc(ws + OFF_MEMV, 0, 64 * 65536, 0x00020000);

    const __amdgpu_buffer_rsrc_t wrs = __builtin_amdgcn_make_buffer_rsrc(ws, 0, (unsigned)WS_END, 0x00020000);
#define ENDSTAMP() do { } while (0)
    if (blockIdx.x >= NCW + NRW) {
        const int wk = blockIdx.x - (NCW + NRW), grp = wk >> 5, wg = wk & 31;
        unsigned cap = SPIN_CAP;
#pragma unroll 1
        for (int i = 0; i < 8; ++i) {
            const int rb = 2 * i + (wk >> 6);
            gemm_core<G_KV, 8>(ws, lds, wrs, rb * 8, wk & 63, wave, [] {});
            arrive(cnt_ptr(ws, rb, 6), wave);
        }
#pragma unroll 1
        for (int u = grp; u < 13; u += 4) worker_unit<8>(x, out, ws, lds, wrs, u, 4 * u, wg, wave, cap);
        {
            const int u = 13 + ((grp + 3) & 3);
            worker_unit<4>(x, out, ws, lds, wrs, u, 52 + 2 * (u - 13), wg, wave, cap);
        }
        if (grp == 1 || grp == 2) worker_unit<2>(x, out, ws, lds, wrs, 16 + grp, 59 + grp, wg, wave, cap);
        ENDSTAMP();
        return;
    }
    if (blockIdx.x >= NCW) {
        const int rw = blockIdx.x - NCW;
        unsigned cap = SPIN_CAP;
        if (wave < RWV) {
        const int r = rw * RWV + wave;
        const int t = r >> 2, gh = (r >> 1) & 1, rh = r & 1;
        const unsigned moff = (lane >> 3) < NCOPY ? (unsigned)((lane >> 3) * 8192 + (t >> 1) * 512 + (((t & 1) * 2 + gh) * 8 + 4 * rh) * 16 + (lane & 7) * 8) : 0x40000000u;
        for (int s = 0; s < NSEG_ - 1; ++s) {
            const unsigned tag = ((s >> 1) + 1) & 1;
            unsigned base = ((s & 1) * 128 + r) * 4096 + lane * 16;
            u32x4 x[4];
            for (unsigned spins = 0;; ++spins) {
                unsigned bad = 0;
                asm volatile("" : "+v"(base)::"memory");
#pragma unroll
                for (int k = 0; k < 4; ++k) x[k] = __builtin_amdgcn_raw_buffer_load_b128(prs, base + k * 1024, 0, 16);
                if (tag) {
                    const u32x4 a4 = (x[0] & x[1]) & (x[2] & x[3]);
                    bad = ~((a4[0] & a4[1]) & (a4[2] & a4[3]));
                } else {
                    const u32x4 o4 = (x[0] | x[1]) | (x[2] | x[3]);
                    bad = (o4[0] | o4[1]) | (o4[2] | o4[3]);
                }
                if (__all((bad & 1u) == 0u)) break;
                if (spins >= (unsigned)__builtin_amdgcn_readfirstlane(cap)) { cap = 0; break; }
            }
            STAMP(0);
            f32x4 a = (__builtin_bit_cast(f32x4, x[0]) + __builtin_bit_cast(f32x4, x[1])) + (__builtin_bit_cast(f32x4, x[2]) + __builtin_bit_cast(f32x4, x[3]));
#pragma unroll
            for (int i = 0; i < 4; ++i) a[i] = add_x32(add_x16(add_x8(a[i])));
            u32x2 o = {pack2(a[0], a[1]), pack2(a[2], a[3])};
            if (o[0] == 0xFFFFFFFFu) o[0] = 0x7FC07FC0u;
            if (o[1] == 0xFFFFFFFFu) o[1] = 0x7FC07FC0u;
            __builtin_amdgcn_raw_buffer_store_b64(o, mrs, (s + 1) * 65536 + moff, 0, 16);
            STAMP(1);
        }
        }
        __syncthreads();
        if (rw < 64) worker_unit<2>(x, out, ws, lds, wrs, 19 + (rw >> 5), 62 + (rw >> 5), rw & 31, wave, cap);
        ENDSTAMP();
        return;
    }
    const int h = blockIdx.x & 31, part = blockIdx.x >> 5;
    const int c = lane & 15, g = lane >> 4;
    bf16x8 wq[2][16], wk[2][16], wv[2][16];
    {
        const char* wb = ws + OFF_WQKV + ((size_t)((h * 8 + wave * 2) * 16) * 64 + lane) * 16;
#pragma unroll
        for (int t = 0; t < 2; ++t)
#pragma unroll
            for (int ks = 0; ks < 16; ++ks) {
                wq[t][ks] = *(const bf16x8*)(wb + (size_t)(t * 16 + ks) * 1024);
                wk[t][ks] = *(const bf16x8*)(wb + 4 * MB + (size_t)(t * 16 + ks) * 1024);
                wv[t][ks] = *(const bf16x8*)(wb + 8 * MB + (size_t)(t * 16 + ks) * 1024);
            }
    }
    for (int i = wave; i < 128 / CPH; i += 4) {
        const int tile = part * (32 / CPH) + (i >> 2), kk = i & 3;
        *(u32x4*)(l_wo + i * 1024 + lane * 16) = *(const u32x4*)(ws + OFF_WO + ((size_t)(tile * 128 + h * 4 + kk) * 64 + lane) * 16);
    }
#pragma unroll
    for (int i = 0; i < 2; ++i) {
        const int idx = tid + i * 256;
        const int row = idx >> 6, ch = idx & 63;
        const u32x4 v = *(const u32x4*)(mem0 + row * 512 + ch * 8);
        *(u32x4*)(l_mem + (ch >> 2) * 512 + ((ch & 3) * 8 + row) * 16) = v;
    }
    const unsigned kfo = (unsigned)(OFF_KF + ((size_t)((h * 4 + wave) * 2) * 64 + lane) * 16);
#define LD_DEV(off) __builtin_bit_cast(bf16x8, __builtin_amdgcn_raw_buffer_load_b128(wrs, (off), 0, 16))
    const unsigned kmo = (unsigned)(OFF_KM + ((size_t)(h * 4 + wave) * 64 + lane) * 16);
    const unsigned vmo = (unsigned)(OFF_VM + ((size_t)((h * 4 + wave) * 2) * 64 + lane) * 8);
    const int mrd = (g * 8 + (c & 7)) * 16;
    const int srd = (c >> 3) * 1024 + (g * 16 + (c & 7)) * 16;
    const int srd2 = 2048 + ((g & 1) * 16 + (c & 7)) * 16 + (2 * (g >> 1) + (c >> 3)) * 4;
    const unsigned poff = c < 8 ? (unsigned)((((part * (32 / CPH) + wave * TPW) * 4 + (g >> 1) * 2 + (c >> 2)) * 32 + h) * 128 + ((c & 3) * 2 + (g & 1)) * 16) : 0x40000000u;
    unsigned cap = SPIN_CAP;
    wait_hint(wrs, (unsigned)(OFF_CNT + 6 * 128), 64, cap);
    bf16x8 kf0 = LD_DEV(kfo), kf1 = LD_DEV(kfo + 1024);
    bf16x8 vf0 = LD_DEV(kfo + 16 * MB), vf1 = LD_DEV(kfo + 16 * MB + 1024);
    __syncthreads();
    unsigned gdel = GDELAY;
#define VACC0(ks) (((ks) & 1) ? aw0 : av0)
#define VACC1(ks) (((ks) & 1) ? aw1 : av1)
#define VSTEP(ks) do { MFMA_VV(VACC0(ks), mf[(ks) & 7], wv[0][ks]); MFMA_VV(VACC1(ks), mf[(ks) & 7], wv[1][ks]); \
                       if ((ks) < 8) mf[(ks) & 7] = *(const bf16x8*)(l_mem + ((ks) + 8) * 512 + mrd); } while (0)
    for (int s = 0; s < NSEG_; ++s) {
        STAMP(0);
        FS(0);
        f32x4 aq0, aq1, ak0, ak1, av0, av1, aw0, aw1;
        bf16x8 mf[8];
#pragma unroll
        for (int ks = 0; ks < 8; ++ks) mf[ks] = *(const bf16x8*)(l_mem + ks * 512 + mrd);
#pragma unroll
        for (int ks = 0; ks < 16; ++ks) {
            const bf16x8 m = mf[ks & 7];
            if (ks == 0) { MFMA_AA0(aq0, wq[0][ks], m); MFMA_AA0(aq1, wq[1][ks], m); MFMA_AA0(ak0, wk[0][ks], m); MFMA_AA0(ak1, wk[1][ks], m); }
            else { MFMA_AA(aq0, wq[0][ks], m); MFMA_AA(aq1, wq[1][ks], m); MFMA_AA(ak0, wk[0][ks], m); MFMA_AA(ak1, wk[1][ks], m); }
            mf[ks & 7] = *(const bf16x8*)(l_mem + ((ks + 8) & 15) * 512 + mrd);
        }
        FS(1);
        MFMA_VV0(av0, mf[0], wv[0][0]); MFMA_VV0(av1, mf[0], wv[1][0]);
        mf[0] = *(const bf16x8*)(l_mem + 8 * 512 + mrd);
        asm volatile("" : "+v"(aq0), "+v"(aq1), "+v"(ak0), "+v"(ak1));
        bf16x8 qB = pack8(aq0, aq1);
        bf16x8 kA = pack8(ak0, ak1);
        NOP_IN2(qB, kA);
        f32x4 s0, s1, s2;
        MFMA_VV0(s0, kf0, qB); MFMA_VV0(s1, kf1, qB); MFMA_VV0(s2, kA, qB);
        MFMA_VV0(aw0, mf[1], wv[0][1]); MFMA_VV0(aw1, mf[1], wv[1][1]);
        mf[1] = *(const bf16x8*)(l_mem + 9 * 512 + mrd);
        asm volatile("" : "+v"(s0), "+v"(s1), "+v"(s2));
        *(f32x4*)(l_sx + (wave * 3 + 0) * 1024 + lane * 16) = s0;
        *(f32x4*)(l_sx + (wave * 3 + 1) * 1024 + lane * 16) = s1;
        *(f32x4*)(l_sx + (wave * 3 + 2) * 1024 + lane * 16) = s2;
        VSTEP(2);
        STAMP(1);
        FS(2);
        __syncthreads();
        FS(3);
        STAMP(2);
        f32x4 sa = *(const f32x4*)(l_sx + 0 * 3072 + srd), sb = *(const f32x4*)(l_sx + 1 * 3072 + srd);
        f32x4 sc = *(const f32x4*)(l_sx + 2 * 3072 + srd), sd = *(const f32x4*)(l_sx + 3 * 3072 + srd);
        float ma = *(const float*)(l_sx + 0 * 3072 + srd2), mb = *(const float*)(l_sx + 1 * 3072 + srd2);
        float mc = *(const float*)(l_sx + 2 * 3072 + srd2), md = *(const float*)(l_sx + 3 * 3072 + srd2);
        VSTEP(3); VSTEP(4); VSTEP(5); VSTEP(6); VSTEP(7); VSTEP(8); VSTEP(9);
        VSTEP(10); VSTEP(11); VSTEP(12); VSTEP(13); VSTEP(14); VSTEP(15);
#define MAX2(a, b) __builtin_amdgcn_fmed3f(a, b, __builtin_inff())
        __builtin_amdgcn_sched_barrier(0);
        FS(4);
        f32x4 e = (sa + sb) + (sc + sd);
        float em = (ma + mb) + (mc + md);
        unsigned r01 = pack2(e[0], e[1]), r23 = pack2(e[2], e[3]), rm = pack2(em, em);
        e[0] = bflo(r01) * SCALE_; e[1] = bfhi(r01) * SCALE_; e[2] = bflo(r23) * SCALE_; e[3] = bfhi(r23) * SCALE_; em = bflo(rm) * SCALE_;
        r01 = pack2(e[0], e[1]); r23 = pack2(e[2], e[3]); rm = pack2(em, em);
        e[0] = bflo(r01); e[1] = bfhi(r01); e[2] = bflo(r23); e[3] = bfhi(r23); em = bflo(rm);
        float mx = MAX2(MAX2(MAX2(e[0], e[1]), MAX2(e[2], e[3])), em);
        mx = MAX2(mx, __uint_as_float(ror8(__float_as_uint(mx))));
        { auto r = __builtin_amdgcn_permlane16_swap(__float_as_uint(mx), __float_as_uint(mx), false, false);
          mx = MAX2(__uint_as_float(r[0]), __uint_as_float(r[1])); }
        { auto r = __builtin_amdgcn_permlane32_swap(__float_as_uint(mx), __float_as_uint(mx), false, false);
          mx = MAX2(__uint_as_float(r[0]), __uint_as_float(r[1])); }
        e[0] = __builtin_amdgcn_exp2f((e[0] - mx) * LOG2E_); e[1] = __builtin_amdgcn_exp2f((e[1] - mx) * LOG2E_);
        e[2] = __builtin_amdgcn_exp2f((e[2] - mx) * LOG2E_); e[3] = __builtin_amdgcn_exp2f((e[3] - mx) * LOG2E_);
        em = __builtin_amdgcn_exp2f((em - mx) * LOG2E_);
        const float sum = add_x32(add_x16(add_x8(((e[0] + e[1]) + (e[2] + e[3])) + em)));
        const float inv = __builtin_amdgcn_rcpf(sum);
        e *= inv; em *= inv;
        FS(5);
        const unsigned p0 = pack2(e[0], e[1]), p1 = pack2(e[2], e[3]);
        const u32x4 pb0u = {p0, p1, ror8(p0), ror8(p1)};
        const unsigned dm = pack2(em, __uint_as_float(ror8(__float_as_uint(em))));
        auto sw = __builtin_amdgcn_permlane32_swap(dm, dm, false, false);
        const u32x2 pb1u = {g < 2 ? sw[0] : 0u, g < 2 ? sw[1] : 0u};
        bf16x8 pB0 = __builtin_bit_cast(bf16x8, pb0u);
        bf16x4 pB1 = __builtin_bit_cast(bf16x4, pb1u);
        NOP_OUT4(av0, av1, aw0, aw1);
        bf16x4 vA0 = pack4(av0 + aw0), vA1 = pack4(av1 + aw1);
        if (s == NSEG_ - 1) {
            if (part == 0) {
                __builtin_amdgcn_raw_buffer_store_b128(__builtin_bit_cast(u32x4, kA), wrs, kmo + s * 131072, 0, 16);
                __builtin_amdgcn_raw_buffer_store_b64(__builtin_bit_cast(u32x2, vA0), wrs, vmo + s * 131072, 0, 16);
                __builtin_amdgcn_raw_buffer_store_b64(__builtin_bit_cast(u32x2, vA1), wrs, vmo + s * 131072 + 512, 0, 16);
                arrive(cnt_ptr(ws, NUNIT - 1, 0), wave);
            }
            break;
        }
        FS(6);
        const char* wop = l_wo + wave * TPW * 4 * 1024 + lane * 16;
        constexpr int NM = TPW * 4;
        bf16x8 wf[8];
#pragma unroll
        for (int i = 0; i < 8; ++i) wf[i] = *(const bf16x8*)(wop + ((i % TPW) * 4 + (i / TPW)) * 1024);
        __builtin_amdgcn_sched_barrier(0);
        bf16x8 pB1x = ext8(pB1), vA0x = ext8(vA0), vA1x = ext8(vA1);
        asm volatile("s_nop 1" : "+v"(pB0), "+v"(pB1x), "+v"(vA0x), "+v"(vA1x));
        f32x4 o0, o1;
        MFMA_VV0(o0, vf0, pB0); MFMA_VV0(o1, vf1, pB0);
        MFMA_VV(o0, vA0x, pB1x); MFMA_VV(o1, vA1x, pB1x);
        NOP_OUT2(o0, o1);
        *(bf16x8*)(l_ox + wave * 1024 + lane * 16) = pack8(o0, o1);
        FS(7);
        __syncthreads();
        FS(8);
        STAMP(3);
        bf16x8 oB[4];
#pragma unroll
        for (int kk = 0; kk < 4; ++kk) oB[kk] = *(const bf16x8*)(l_ox + kk * 1024 + lane * 16);
        const unsigned tag = ((s >> 1) + 1) & 1;
        const unsigned pbase = (s & 1) * 128 * 4096 + poff;
        f32x4 pa[TPW];
#pragma unroll
        for (int i = 0; i < NM; ++i) {
            const int kk = i / TPW, t = i % TPW;
            const bf16x8 w = wf[i & 7];
            if (kk == 0) MFMA_VV0(pa[t], w, oB[kk]); else MFMA_VV(pa[t], w, oB[kk]);
            if (i + 8 < NM) { const int n = i + 8; wf[i & 7] = *(const bf16x8*)(wop + ((n % TPW) * 4 + (n / TPW)) * 1024); }
        }
        FS(9);
        NOP_OUT2(pa[TPW - 2], pa[TPW - 1]);
        {
            u32x4 pu[TPW];
#pragma unroll
            for (int q = 0; q < TPW; ++q) {
                asm volatile("" : "+v"(pa[q]));
                pu[q] = (__builtin_bit_cast(u32x4, pa[q]) & ~1u) | tag;
            }
#pragma unroll
            for (int q = 0; q < TPW; ++q) asm volatile("" : "+v"(pu[q]));
#pragma unroll
            for (int q = 0; q < TPW; ++q) __builtin_amdgcn_raw_buffer_store_b128(pu[q], prs, pbase, q * 4 * 4096, 16);
        }
        FS(10);
        if (part == 0) {
            __builtin_amdgcn_raw_buffer_store_b128(__builtin_bit_cast(u32x4, kA), wrs, kmo + s * 131072, 0, 16);
            __builtin_amdgcn_raw_buffer_store_b64(__builtin_bit_cast(u32x2, vA0), wrs, vmo + s * 131072, 0, 16);
            __builtin_amdgcn_raw_buffer_store_b64(__builtin_bit_cast(u32x2, vA1), wrs, vmo + s * 131072 + 512, 0, 16);
        }
        {
            if ((s & 3) == 3) wait_hint(wrs, (unsigned)(OFF_CNT + (((s + 1) >> 2) * 8 + 6) * 128), 64, cap);
            const unsigned so = kfo + (unsigned)(s + 1) * 32 * 8 * 1024;
            kf0 = LD_DEV(so); kf1 = LD_DEV(so + 1024);
            vf0 = LD_DEV(so + 16 * MB); vf1 = LD_DEV(so + 16 * MB + 1024);
        }
        STAMP(4);
        FS(11);
        {
            unsigned off = (s + 1) * 65536 + (blockIdx.x % NCOPY) * 8192 + wave * 2048 + lane * 16;
            u32x4 y0, y1;
            for (unsigned spins = 0;; ++spins) {
                asm volatile("" : "+v"(off)::"memory");
                y0 = __builtin_amdgcn_raw_buffer_load_b128(mrs, off, 0, 16);
                y1 = __builtin_amdgcn_raw_buffer_load_b128(mrs, off + 1024, 0, 16);
                const unsigned mxu = max(max(max(y0[0], y0[1]), max(y0[2], y0[3])), max(max(y1[0], y1[1]), max(y1[2], y1[3])));
                const bool ok = mxu != 0xFFFFFFFFu;
                if (__all(ok)) {
                    break;
                }
                if (spins >= (unsigned)__builtin_amdgcn_readfirstlane(cap)) { cap = 0; break; }
            }
            STAMP(5);
            *(u32x4*)(l_mem + wave * 2048 + lane * 16) = y0;
            *(u32x4*)(l_mem + wave * 2048 + 1024 + lane * 16) = y1;
        }
        asm volatile("s_waitcnt vmcnt(0)" ::: "memory");
        __syncthreads();
        if (part == 0 && wave == 0 && get_lane() == 0) {
            const int u = unit_of_last_seg(s);
            if (u >= 0) __hip_atomic_fetch_add(cnt_ptr(ws, u, 0), 1u, __ATOMIC_RELAXED, __HIP_MEMORY_SCOPE_AGENT);
        }
    }
}

extern "C" void kernel_launch(void* const* d_in, const int* in_sizes, int n_in, void* d_out, int out_size, void* d_ws,
                              size_t ws_size, hipStream_t stream) {
    const unsigned short* x = (const unsigned short*)d_in[0];
    const unsigned short* mem0 = (const unsigned short*)d_in[1];
    const unsigned short* Wq = (const unsigned short*)d_in[2];
    const unsigned short* Wk = (const unsigned short*)d_in[3];
    const unsigned short* Wv = (const unsigned short*)d_in[4];
    const unsigned short* Wo = (const unsigned short*)d_in[5];
    const unsigned short* Wg = (const unsigned short*)d_in[8];
    const unsigned short* Wu = (const unsigned short*)d_in[9];
    const unsigned short* Wd = (const unsigned short*)d_in[10];
    char* ws = (char*)d_ws;
    prep_kernel<<<NB_PREP, 256, 0, stream>>>(x, Wq, Wk, Wv, Wo, Wg, Wu, Wd, ws);
    chain_kernel<<<NCW + NRW + NWK, 256, 0, stream>>>(mem0, x, (unsigned short*)d_out, ws);
}
```

```cpp
#include <hip/hip_runtime.h>
#include <hip/hip_bf16.h>
#include <stdint.h>
#include <type_traits>

constexpr int S_ = 2048, D_ = 512, H_ = 32, DH_ = 128, M_ = 8, SEG_ = 32, NSEG_ = 64, F_ = 1792;

typedef short bf16x8 __attribute__((ext_vector_type(8)));
typedef short bf16x4 __attribute__((ext_vector_type(4)));
typedef float f32x4 __attribute__((ext_vector_type(4)));
typedef unsigned u32x4 __attribute__((ext_vector_type(4)));
typedef unsigned u32x2 __attribute__((ext_vector_type(2)));
typedef __bf16 bfv2 __attribute__((ext_vector_type(2)));

__device__ __forceinline__ unsigned pack2(float a, float b) {
    bfv2 v = {(__bf16)a, (__bf16)b};
    return __builtin_bit_cast(unsigned, v);
}
__device__ __forceinline__ float bf16r(float a) { return (float)(__bf16)a; }
__device__ __forceinline__ float bflo(unsigned u) { return __uint_as_float(u << 16); }
__device__ __forceinline__ float bfhi(unsigned u) { return __uint_as_float(u & 0xffff0000u); }
__device__ __forceinline__ bf16x8 pack8(f32x4 a, f32x4 b) {
    u32x4 r = {pack2(a[0], a[1]), pack2(a[2], a[3]), pack2(b[0], b[1]), pack2(b[2], b[3])};
    return __builtin_bit_cast(bf16x8, r);
}
__device__ __forceinline__ bf16x4 pack4(f32x4 a) {
    u32x2 r = {pack2(a[0], a[1]), pack2(a[2], a[3])};
    return __builtin_bit_cast(bf16x4, r);
}
__device__ __forceinline__ int get_lane() {
    int l;
    asm volatile("v_mbcnt_lo_u32_b32 %0, -1, 0\n\tv_mbcnt_hi_u32_b32 %0, -1, %0" : "=v"(l));
    return l;
}
#define MFMA32(a, b, c) __builtin_amdgcn_mfma_f32_16x16x32_bf16(a, b, c, 0, 0, 0)
__device__ __forceinline__ bf16x8 ext8(bf16x4 a) { bf16x8 r = {a[0], a[1], a[2], a[3], 0, 0, 0, 0}; return r; }
#define MFMA16(a, b, c) MFMA32(ext8(a), ext8(b), c)

constexpr size_t MB = 1024 * 1024;
constexpr size_t OFF_WQKV = 0;
constexpr size_t OFF_WO = OFF_WQKV + 12 * MB;
constexpr size_t OFF_WGU = OFF_WO + 4 * MB;
constexpr size_t OFF_WD = OFF_WGU + 4 * MB;
constexpr size_t OFF_XN = OFF_WD + 2 * MB;
constexpr size_t OFF_QF = OFF_XN + 2 * MB;
constexpr size_t OFF_KF = OFF_QF + 16 * MB;
constexpr size_t OFF_VF = OFF_KF + 16 * MB;
constexpr size_t OFF_KM = OFF_VF + 16 * MB;
constexpr size_t OFF_VM = OFF_KM + 8 * MB;
constexpr size_t OFF_OF = OFF_VM + 8 * MB;
constexpr size_t OFF_P1 = OFF_OF + 16 * MB;
constexpr size_t OFF_H = OFF_P1 + 32 * MB;
constexpr size_t OFF_HN = OFF_H + 2 * MB;
constexpr size_t OFF_AF = OFF_HN + 2 * MB;
constexpr size_t OFF_P2 = OFF_AF + 8 * MB;
constexpr size_t OFF_PART = OFF_P2 + 16 * MB;
constexpr size_t OFF_MEMV = OFF_PART + 1 * MB;
constexpr size_t OFF_CNT = OFF_MEMV + 4 * MB;
constexpr size_t WS_END = OFF_CNT + 1 * MB;

__device__ __forceinline__ float wave_sum(float v) {
#pragma unroll
    for (int m = 1; m < 64; m <<= 1) v += __shfl_xor(v, m);
    return v;
}
__device__ __forceinline__ u32x4 ln_row(const float (&x)[8]) {
    float s = 0.f;
#pragma unroll
    for (int j = 0; j < 8; ++j) s += x[j];
    const float mean = wave_sum(s) * (1.0f / 512.0f);
    float q = 0.f;
#pragma unroll
    for (int j = 0; j < 8; ++j) { const float d = x[j] - mean; q += d * d; }
    const float var = wave_sum(q) * (1.0f / 512.0f);
    const float mb = bf16r(mean);
    const float vb = bf16r(var);
    const float den = bf16r(sqrtf(bf16r(vb + 1e-5f)));
    float y[8];
#pragma unroll
    for (int j = 0; j < 8; ++j) y[j] = bf16r(x[j] - mb) / den;
    u32x4 r = {pack2(y[0], y[1]), pack2(y[2], y[3]), pack2(y[4], y[5]), pack2(y[6], y[7])};
    return r;
}
__device__ __forceinline__ void store_row_frag(char* base, int row, int lane, u32x4 v) {
    const int tt = row >> 4, c = row & 15, ks = lane >> 2, g = lane & 3;
    *(u32x4*)(base + ((size_t)(tt * 16 + ks) * 64 + g * 16 + c) * 16) = v;
}

constexpr int NB_QKV = 3 * 256, NB_WO = 256, NB_GU = 2 * 112, NB_WD = 112, NB_LN = 512, NB_CLR = 64 + 256 + 4;
constexpr int NB_PREP = NB_QKV + NB_WO + NB_GU + NB_WD + NB_LN + NB_CLR;

__global__ void __launch_bounds__(256) prep_kernel(const unsigned short* __restrict__ x, const unsigned short* __restrict__ Wq,
                                                   const unsigned short* __restrict__ Wk, const unsigned short* __restrict__ Wv,
                                                   const unsigned short* __restrict__ Wo, const unsigned short* __restrict__ Wg,
                                                   const unsigned short* __restrict__ Wu, const unsigned short* __restrict__ Wd,
                                                   char* __restrict__ ws) {
    __shared__ __attribute__((aligned(16))) unsigned short t[32][264];
    int b = blockIdx.x;
    const int tid = threadIdx.x, wave = tid >> 6, lane = tid & 63;
    const unsigned short* src; int N, ks, nb, KS; bool perm = false; char* dst; int mode = 0;
    if (b < NB_QKV) {
        const int m = b >> 8; b &= 255;
        src = m == 0 ? Wq : (m == 1 ? Wk : Wv); N = 4096; KS = 16; ks = b & 15; nb = b >> 4; dst = ws + OFF_WQKV + (size_t)m * 4 * MB;
    } else if (b < NB_QKV + NB_WO) {
        b -= NB_QKV; src = Wo; N = 512; KS = 128; ks = b & 127; nb = b >> 7; perm = true; dst = ws + OFF_WO;
    } else if (b < NB_QKV + NB_WO + NB_GU) {
        b -= NB_QKV + NB_WO; const int up = b >= 112; if (up) b -= 112;
        src = up ? Wu : Wg; N = 1792; KS = 16; ks = b & 15; nb = b >> 4; dst = ws + OFF_WGU; mode = 1 + up;
    } else if (b < NB_QKV + NB_WO + NB_GU + NB_WD) {
        b -= NB_QKV + NB_WO + NB_GU; src = Wd; N = 512; KS = 56; ks = b % 56; nb = b / 56; perm = true; dst = ws + OFF_WD;
    } else if (b < NB_QKV + NB_WO + NB_GU + NB_WD + NB_LN) {
        b -= NB_QKV + NB_WO + NB_GU + NB_WD;
        const int row = b * 4 + wave;
        const u32x4 v = *(const u32x4*)(x + (size_t)row * 512 + lane * 8);
        float f[8] = {bflo(v[0]), bfhi(v[0]), bflo(v[1]), bfhi(v[1]), bflo(v[2]), bfhi(v[2]), bflo(v[3]), bfhi(v[3])};
        store_row_frag(ws + OFF_XN, row, lane, ln_row(f));
        return;
    } else {
        b -= NB_QKV + NB_WO + NB_GU + NB_WD + NB_LN;
        const bool p = b < 64 || b >= 320;
        char* d = b < 64 ? ws + OFF_PART + (size_t)b * 16384 : ws + OFF_MEMV + (size_t)(b - 64) * 16384;
        const unsigned f = p ? 0u : 0xFFFFFFFFu;
        const u32x4 v = {f, f, f, f};
#pragma unroll
        for (int i = 0; i < 4; ++i) *(u32x4*)(d + i * 4096 + tid * 16) = v;
        return;
    }
    {
        u32x4 v[4];
#pragma unroll
        for (int i = 0; i < 4; ++i) v[i] = *(const u32x4*)(src + (size_t)(ks * 32 + i * 8 + (tid >> 5)) * N + nb * 256 + (tid & 31) * 8);
#pragma unroll
        for (int i = 0; i < 4; ++i) *(u32x4*)&t[i * 8 + (tid >> 5)][(tid & 31) * 8] = v[i];
    }
    __syncthreads();
    const int c = lane & 15, g = lane >> 4;
#pragma unroll
    for (int q = 0; q < 4; ++q) {
        unsigned short e[8];
#pragma unroll
        for (int j = 0; j < 8; ++j) {
            const int k = perm ? (j < 4 ? 4 * g + j : 16 + 4 * g + j - 4) : 8 * g + j;
            e[j] = t[k][(q * 4 + wave) * 16 + c];
        }
        int nt = nb * 16 + q * 4 + wave;
        if (mode) nt = (nt >> 1) * 4 + (mode == 2 ? 2 : 0) + (nt & 1);
        u32x4 o = {e[0] | ((unsigned)e[1] << 16), e[2] | ((unsigned)e[3] << 16), e[4] | ((unsigned)e[5] << 16), e[6] | ((unsigned)e[7] << 16)};
        *(u32x4*)(dst + ((size_t)(nt * KS + ks) * 64 + lane) * 16) = o;
    }
}

enum { G_KV = 0, G_OUTP = 1, G_FFN1 = 2, G_DOWN = 3, G_Q = 4 };

__device__ __forceinline__ void st16_dev(const __amdgpu_buffer_rsrc_t rs, size_t off, u32x4 v) {
    __builtin_amdgcn_raw_buffer_store_b128(v, rs, (unsigned)off, 0, 16);
}

template <int MODE, int TT, class SYNC>
__device__ __forceinline__ void gemm_core(char* ws, char* xl, const __amdgpu_buffer_rsrc_t wrs, int tt0, int cb, int wave, SYNC sync) {
    int lane;
    constexpr int NKS = MODE == G_DOWN ? 14 : 16;
    constexpr int KST = MODE == G_OUTP ? 128 : (MODE == G_DOWN ? 56 : 16);
    constexpr int NT = MODE == G_FFN1 ? 4 : 2;
    constexpr int NIT = 1;
    lane = get_lane();
    int ks0 = 0, cg = cb;
    const char* xsrc; unsigned wsrc;
    if (MODE == G_KV || MODE == G_Q) { xsrc = ws + OFF_XN; wsrc = (unsigned)OFF_WQKV; }
    else if (MODE == G_OUTP) { xsrc = ws + OFF_OF; wsrc = (unsigned)OFF_WO; ks0 = (cb & 7) * 16; cg = cb >> 3; }
    else if (MODE == G_FFN1) { xsrc = ws + OFF_HN; wsrc = (unsigned)OFF_WGU; }
    else { xsrc = ws + OFF_AF; wsrc = (unsigned)OFF_WD; ks0 = (cb & 3) * 14; cg = cb >> 2; }
    const int c = lane & 15, g = lane >> 4;
    auto tile0 = [&](int it) -> int {
        if (MODE == G_KV) { const int ci = 32 + cg; return (ci >> 5) * 256 + (ci & 31) * 8 + wave * 2; }
        else if (MODE == G_Q) return cg * 8 + wave * 2;
        else if (MODE == G_FFN1) return (cg * 4 + wave) * 4;
        else return cg * 8 + wave * 2;
    };
    constexpr int PD = NKS / 2;
    bf16x8 wr[PD][NT];
    const unsigned wvo = wsrc + (unsigned)((tile0(0) * KST + ks0) * 64 + lane) * 16;
#define LD_W(t, ks) __builtin_bit_cast(bf16x8, __builtin_amdgcn_raw_buffer_load_b128(wrs, wvo, ((t) * KST + (ks)) * 1024, 0))
#pragma unroll
    for (int ks = 0; ks < PD; ++ks)
#pragma unroll
        for (int t = 0; t < NT; ++t) wr[ks][t] = LD_W(t, ks);
    sync();
#pragma unroll
    for (int j = 0; j < (TT * NKS + 3) / 4; ++j) {
        const int i = j * 4 + wave;
        if (i < TT * NKS) {
            const int tt = i / NKS, ks = i % NKS;
            if constexpr (MODE == G_KV || MODE == G_Q)
                __builtin_amdgcn_global_load_lds((const unsigned*)(xsrc + ((size_t)((tt0 + tt) * KST + ks0 + ks) * 64 + lane) * 16),
                                                 (unsigned*)(xl + (size_t)i * 1024 + lane * 16), 16, 0, 0);
            else
                __builtin_amdgcn_global_load_lds((const unsigned*)(xsrc + ((size_t)((tt0 + tt) * KST + ks0 + ks) * 64 + lane) * 16),
                                                 (unsigned*)(xl + (size_t)i * 1024 + lane * 16), 16, 0, 16);
        }
    }
    asm volatile("s_waitcnt vmcnt(0)" ::: "memory");
    __syncthreads();
#pragma unroll 1
    for (int it = 0; it < NIT; ++it) {
        asm volatile("" ::: "memory");
        const int nt0 = tile0(it);
        int mat = 0, head = 0;
        if (MODE == G_KV) { const int ci = 32 + cg; mat = ci >> 5; head = ci & 31; }
        f32x4 acc[NT][TT];
#pragma unroll
        for (int t = 0; t < NT; ++t)
#pragma unroll
            for (int tt = 0; tt < TT; ++tt) acc[t][tt] = (f32x4){0.f, 0.f, 0.f, 0.f};
        const bool vmat = (MODE == G_KV) && (mat == 2);
        auto kloop = [&](auto swapped) {
            bf16x8 xf[2][TT];
#pragma unroll
            for (int tt = 0; tt < TT; ++tt) xf[0][tt] = *(const bf16x8*)(xl + (tt * NKS) * 1024 + lane * 16);
#pragma unroll
            for (int ks = 0; ks < NKS; ++ks) {
                bf16x8 wf[NT];
#pragma unroll
                for (int t = 0; t < NT; ++t) {
                    wf[t] = wr[ks % PD][t];
                    if (ks + PD < NKS) wr[ks % PD][t] = LD_W(t, ks + PD);
                }
                if (ks + 1 < NKS) {
#pragma unroll
                    for (int tt = 0; tt < TT; ++tt) xf[(ks + 1) & 1][tt] = *(const bf16x8*)(xl + (tt * NKS + ks + 1) * 1024 + lane * 16);
                }
                __builtin_amdgcn_sched_barrier(0);
#pragma unroll
                for (int tt = 0; tt < TT; ++tt)
#pragma unroll
                    for (int t = 0; t < NT; ++t) {
                        if constexpr (decltype(swapped)::value) acc[t][tt] = MFMA32(xf[ks & 1][tt], wf[t], acc[t][tt]);
                        else acc[t][tt] = MFMA32(wf[t], xf[ks & 1][tt], acc[t][tt]);
                    }
                __builtin_amdgcn_sched_barrier(0);
            }
        };
        if (vmat) kloop(std::true_type{}); else kloop(std::false_type{});
        if (MODE == G_KV) {
            if (mat == 2) {
#pragma unroll
                for (int sg = 0; sg < TT / 2; ++sg)
#pragma unroll
                    for (int t = 0; t < 2; ++t) {
                        const int seg = tt0 / 2 + sg;
                        st16_dev(wrs, OFF_VF + ((size_t)(((seg * 32 + head) * 4 + wave) * 2 + t) * 64 + lane) * 16, __builtin_bit_cast(u32x4, pack8(acc[t][2 * sg], acc[t][2 * sg + 1])));
                    }
            } else {
#pragma unroll
                for (int tt = 0; tt < TT; ++tt) {
                    const int seg = tt0 / 2 + (tt >> 1);
                    st16_dev(wrs, OFF_KF + ((size_t)(((seg * 32 + head) * 4 + wave) * 2 + (tt & 1)) * 64 + lane) * 16, __builtin_bit_cast(u32x4, pack8(acc[0][tt], acc[1][tt])));
                }
            }
        } else if (MODE == G_Q) {
            __syncthreads();
#pragma unroll
            for (int tt = 0; tt < TT; ++tt) *(bf16x8*)(xl + (tt * 4 + wave) * 1024 + lane * 16) = pack8(acc[0][tt], acc[1][tt]);
        } else if (MODE == G_OUTP || MODE == G_DOWN) {
            const size_t dst = (MODE == G_OUTP ? OFF_P1 : OFF_P2) + (size_t)(MODE == G_OUTP ? (cb & 7) : (cb & 3)) * S_ * D_ * 4;
#pragma unroll
            for (int t = 0; t < 2; ++t)
#pragma unroll
                for (int tt = 0; tt < TT; ++tt)
                    st16_dev(wrs, dst + ((size_t)((tt0 + tt) * 16 + c) * D_ + (nt0 + t) * 16 + g * 4) * 4, __builtin_bit_cast(u32x4, acc[t][tt]));
        } else {
            const int ag = cg * 4 + wave;
#pragma unroll
            for (int tt = 0; tt < TT; ++tt) {
                f32x4 a0, a1;
#pragma unroll
                for (int i = 0; i < 4; ++i) {
                    a0[i] = bf16r(acc[0][tt][i]) * bf16r(acc[2][tt][i]);
                    a1[i] = bf16r(acc[1][tt][i]) * bf16r(acc[NT - 1][tt][i]);
                }
                st16_dev(wrs, OFF_AF + ((size_t)((tt0 + tt) * 56 + ag) * 64 + lane) * 16, __builtin_bit_cast(u32x4, pack8(a0, a1)));
            }
        }
    }
}

constexpr float SCALE_ = 0.08838834764831845f;
constexpr float LOG2E_ = 1.4426950408889634f;
constexpr unsigned SPIN_CAP = 1u << 17;

#define FS(i) do { } while (0)
#define STAMP(i) do { } while (0)
__device__ __forceinline__ float max_x16(float v) {
    auto r = __builtin_amdgcn_permlane16_swap(__float_as_uint(v), __float_as_uint(v), false, false);
    return fmaxf(__uint_as_float(r[0]), __uint_as_float(r[1]));
}
__device__ __forceinline__ float max_x32(float v) {
    auto r = __builtin_amdgcn_permlane32_swap(__float_as_uint(v), __float_as_uint(v), false, false);
    return fmaxf(__uint_as_float(r[0]), __uint_as_float(r[1]));
}
__device__ __forceinline__ float add_x16(float v) {
    auto r = __builtin_amdgcn_permlane16_swap(__float_as_uint(v), __float_as_uint(v), false, false);
    return __uint_as_float(r[0]) + __uint_as_float(r[1]);
}
__device__ __forceinline__ float add_x32(float v) {
    auto r = __builtin_amdgcn_permlane32_swap(__float_as_uint(v), __float_as_uint(v), false, false);
    return __uint_as_float(r[0]) + __uint_as_float(r[1]);
}
__device__ __forceinline__ float add_x8(float v) {
    return v + __uint_as_float(__builtin_amdgcn_update_dpp(0u, __float_as_uint(v), 0x128, 0xf, 0xf, false));
}
#define MFMA_AA(acc, wa, b) asm volatile("v_mfma_f32_16x16x32_bf16 %0, %1, %2, %0" : "+v"(acc) : "a"(wa), "v"(b))
#define MFMA_AA0(acc, wa, b) asm volatile("v_mfma_f32_16x16x32_bf16 %0, %1, %2, 0" : "=&v"(acc) : "a"(wa), "v"(b))
#define MFMA_VV(acc, a, b) asm volatile("v_mfma_f32_16x16x32_bf16 %0, %1, %2, %0" : "+v"(acc) : "v"(a), "v"(b))
#define MFMA_VV0(acc, a, b) asm volatile("v_mfma_f32_16x16x32_bf16 %0, %1, %2, 0" : "=&v"(acc) : "v"(a), "v"(b))
#define NOP_IN2(a, b) asm volatile("s_nop 1" : "+v"(a), "+v"(b))
#define NOP_OUT2(a, b) asm volatile("s_nop 7\n\ts_nop 3" : "+v"(a), "+v"(b))
#define NOP_OUT4(a, b, c, d) asm volatile("s_nop 7\n\ts_nop 3" : "+v"(a), "+v"(b), "+v"(c), "+v"(d))

#define CPH 2
constexpr int NCW = 32 * CPH;
constexpr int TPW = 8 / CPH;
__device__ __forceinline__ float max_x8(float v) {
    return fmaxf(v, __uint_as_float(__builtin_amdgcn_update_dpp(0u, __float_as_uint(v), 0x128, 0xf, 0xf, false)));
}
__device__ __forceinline__ unsigned ror8(unsigned v) { return __builtin_amdgcn_update_dpp(0u, v, 0x128, 0xf, 0xf, false); }

#define HINTS 0
#define HPOS 2
#define DROPL 1
#define GDELAY 0
#define H2POS 1
#define HDELAY 0
#define NCOPY 8
#define GDEC 1
#define GINC 15
#define GMIN 60
#define SLBIG 32
#define SLCH 127
#define GP 360
#define GE 25
#define GG 108
#define RLO 20
#define CHI 60
#define GK 2
#define SLMID 20
#define RWV 2
constexpr int NRW = 128 / RWV;
constexpr int NWK = 128;
constexpr int NUNIT = 21;
__device__ __forceinline__ int unit_of_last_seg(int s) {
    if (s < 52) return (s & 3) == 3 ? (s >> 2) : -1;
    if (s < 60) return (s & 1) ? 13 + ((s - 52) >> 1) : -1;
    return 17 + (s - 60);
}
constexpr unsigned HINT1 = (unsigned)(OFF_CNT + 32768), HINT2 = (unsigned)(OFF_CNT + 32768 + 16384);
__device__ __forceinline__ void wait_hint(const __amdgpu_buffer_rsrc_t rs, unsigned off, unsigned want, unsigned cap) {
    cap = (unsigned)__builtin_amdgcn_readfirstlane(cap);
    for (unsigned spins = 0; spins <= cap; ++spins) {
        asm volatile("" : "+v"(off)::"memory");
        const unsigned v = __builtin_amdgcn_raw_buffer_load_b32(rs, off, 0, 16);
        if ((unsigned)__builtin_amdgcn_readfirstlane(v) >= want) break;
    }
}
__device__ __forceinline__ void wait_grid(unsigned t0, unsigned off, unsigned lo, unsigned hi) {
    constexpr unsigned DL = GP / GK;
    const unsigned t = (unsigned)__builtin_amdgcn_s_memrealtime() - t0;
    const unsigned d = lo + DL - 1 - (t + lo + 8 * GP + DL - 1 - off) % DL;
    if (d <= hi) {
        const unsigned target = t + d;
        while ((unsigned)__builtin_amdgcn_s_memrealtime() - t0 < target) { }
    }
}
typedef __attribute__((address_space(1))) unsigned gu32;
__device__ __forceinline__ gu32* cnt_ptr(char* ws, int u, int ph) { return (gu32*)(ws + OFF_CNT + (size_t)(u * 8 + ph) * 128); }
__device__ __forceinline__ void arrive(gu32* c, int wave) {
    asm volatile("s_waitcnt vmcnt(0)" ::: "memory");
    __syncthreads();
    if (wave == 0 && get_lane() == 0) __hip_atomic_fetch_add(c, 1u, __ATOMIC_RELAXED, __HIP_MEMORY_SCOPE_AGENT);
}
template <int SLP>
__device__ __forceinline__ void wait_for(gu32* c, unsigned target, int wave, unsigned& cap) {
    if (wave == 0) {
        for (unsigned spins = 0;; ++spins) {
            if ((unsigned)__builtin_amdgcn_readfirstlane(__hip_atomic_load(c, __ATOMIC_RELAXED, __HIP_MEMORY_SCOPE_AGENT)) >= target) break;
            if (spins >= cap) { cap = 0; break; }
            __builtin_amdgcn_s_sleep(SLP);
        }
    }
    asm volatile("" ::: "memory");
    __syncthreads();
}

#define LD16_DEV(off) __builtin_bit_cast(bf16x8, __builtin_amdgcn_raw_buffer_load_b128(wrs, (unsigned)(off), 0, 16))
#define LD8_DEV(off) __builtin_bit_cast(bf16x4, __builtin_amdgcn_raw_buffer_load_b64(wrs, (unsigned)(off), 0, 16))
__device__ __forceinline__ void seg_attention(const char* qf, const __amdgpu_buffer_rsrc_t wrs, int seg, int h,
                                              const bf16x8 (&kf)[8], const bf16x8 (&vf)[8]) {
    const int lane = get_lane();
    const int c = lane & 15, g = lane >> 4;
    bf16x8 km[4]; bf16x4 vm[8];
#pragma unroll
    for (int w = 0; w < 4; ++w) km[w] = LD16_DEV(OFF_KM + ((size_t)((seg * 32 + h) * 4 + w) * 64 + lane) * 16);
#pragma unroll
    for (int i = 0; i < 8; ++i) vm[i] = LD8_DEV(OFF_VM + ((size_t)((seg * 32 + h) * 8 + i) * 64 + lane) * 8);
    __builtin_amdgcn_sched_barrier(0);
    const f32x4 z = {0, 0, 0, 0};
    f32x4 sc[2][3];
#pragma unroll
    for (int qt = 0; qt < 2; ++qt)
#pragma unroll
        for (int kt = 0; kt < 3; ++kt) sc[qt][kt] = z;
    bf16x8 q[2][4];
#pragma unroll
    for (int w = 0; w < 4; ++w) {
        q[0][w] = *(const bf16x8*)(qf + (0 * 4 + w) * 1024 + lane * 16);
        q[1][w] = *(const bf16x8*)(qf + (1 * 4 + w) * 1024 + lane * 16);
        sc[0][0] = MFMA32(kf[w * 2], q[0][w], sc[0][0]); sc[0][1] = MFMA32(kf[w * 2 + 1], q[0][w], sc[0][1]);
        sc[1][0] = MFMA32(kf[w * 2], q[1][w], sc[1][0]); sc[1][1] = MFMA32(kf[w * 2 + 1], q[1][w], sc[1][1]);
    }
#pragma unroll
    for (int w = 0; w < 4; ++w) { sc[0][2] = MFMA32(km[w], q[0][w], sc[0][2]); sc[1][2] = MFMA32(km[w], q[1][w], sc[1][2]); }
    bf16x8 pB0[2]; bf16x4 pB1[2];
#pragma unroll
    for (int qt = 0; qt < 2; ++qt) {
        const int qi = qt * 16 + c;
        float mx = -3.0e38f;
#pragma unroll
        for (int kt = 0; kt < 3; ++kt)
#pragma unroll
            for (int i = 0; i < 4; ++i) {
                float v = bf16r(bf16r(sc[qt][kt][i]) * SCALE_);
                const bool valid = kt == 2 ? (g < 2) : (kt * 16 + 4 * g + i <= qi);
                v = valid ? v : -3.0e38f;
                sc[qt][kt][i] = v;
                mx = fmaxf(mx, v);
            }
        mx = max_x32(max_x16(mx));
        float sum = 0.f;
#pragma unroll
        for (int kt = 0; kt < 3; ++kt)
#pragma unroll
            for (int i = 0; i < 4; ++i) {
                const bool valid = kt == 2 ? (g < 2) : (kt * 16 + 4 * g + i <= qi);
                const float e = valid ? __builtin_amdgcn_exp2f((sc[qt][kt][i] - mx) * LOG2E_) : 0.f;
                sc[qt][kt][i] = e;
                sum += e;
            }
        sum = add_x32(add_x16(sum));
        const float inv = __builtin_amdgcn_rcpf(sum);
#pragma unroll
        for (int kt = 0; kt < 3; ++kt) sc[qt][kt] *= inv;
        pB0[qt] = pack8(sc[qt][0], sc[qt][1]);
        pB1[qt] = pack4(sc[qt][2]);
    }
#pragma unroll
    for (int w = 0; w < 4; ++w) {
#pragma unroll
        for (int qt = 0; qt < 2; ++qt) {
            f32x4 o0 = MFMA32(vf[w * 2], pB0[qt], z);
            f32x4 o1 = MFMA32(vf[w * 2 + 1], pB0[qt], z);
            o0 = MFMA16(vm[w * 2], pB1[qt], o0);
            o1 = MFMA16(vm[w * 2 + 1], pB1[qt], o1);
            st16_dev(wrs, OFF_OF + ((size_t)((seg * 2 + qt) * 128 + h * 4 + w) * 64 + lane) * 16, __builtin_bit_cast(u32x4, pack8(o0, o1)));
        }
    }
}

#define WSTAMP(i) do { } while (0)
template <int TT, bool FAST>
__device__ __forceinline__ void worker_unit(const unsigned short* x, unsigned short* out, char* ws, char* lds, const __amdgpu_buffer_rsrc_t wrs,
                                            int u, int seg0, int wg, int wave, unsigned& cap) {
    constexpr int SL = FAST ? 2 : (TT > 4 ? SLBIG : SLMID);
    const int tt0 = seg0 * 2;
    WSTAMP(0);
    gemm_core<G_Q, TT>(ws, lds, wrs, tt0, wg, wave, [] {});
    WSTAMP(1);
    {
        wait_for<16>(cnt_ptr(ws, seg0 >> 2, 6), 64, wave, cap);
        bf16x8 kf[8], vf[8];
        const int sw = wave < TT / 2 ? wave : 0;
        {
            const int lane = get_lane();
            const unsigned fb = (unsigned)(OFF_KF + ((size_t)((seg0 + sw) * 32 + wg) * 8 * 64 + lane) * 16);
#pragma unroll
            for (int i = 0; i < 8; ++i) { kf[i] = LD16_DEV(fb + i * 1024); vf[i] = LD16_DEV(fb + 16 * MB + i * 1024); }
        }
        wait_for<(FAST ? 2 : (TT > 4 ? SLCH : 32))>(cnt_ptr(ws, u, 0), 32, wave, cap);
        WSTAMP(2);
        if (wave < TT / 2) seg_attention(lds + wave * 8192, wrs, seg0 + wave, wg, kf, vf);
    }
    WSTAMP(3);
    arrive(cnt_ptr(ws, u, 1), wave);
    gemm_core<G_OUTP, TT>(ws, lds, wrs, tt0, wg, wave, [&] { wait_for<SL>(cnt_ptr(ws, u, 1), 32, wave, cap); WSTAMP(4); });
    WSTAMP(5);
    arrive(cnt_ptr(ws, u, 2), wave);
    wait_for<SL>(cnt_ptr(ws, u, 2), 32, wave, cap);
    WSTAMP(6);
    const int lrow = wg * 4 + wave;
    const int row = tt0 * 16 + lrow;
    if (lrow < TT * 16) {
        const int lane = get_lane();
        const unsigned po = (unsigned)(OFF_P1 + ((size_t)row * 512 + lane * 8) * 4);
        f32x4 pv[16];
#pragma unroll
        for (int k = 0; k < 8; ++k) {
            pv[2 * k] = __builtin_bit_cast(f32x4, __builtin_amdgcn_raw_buffer_load_b128(wrs, po, k * S_ * D_ * 4, 16));
            pv[2 * k + 1] = __builtin_bit_cast(f32x4, __builtin_amdgcn_raw_buffer_load_b128(wrs, po + 16, k * S_ * D_ * 4, 16));
        }
        const u32x4 v = *(const u32x4*)(x + (size_t)row * 512 + lane * 8);
        __builtin_amdgcn_sched_barrier(0);
        f32x4 a0 = pv[0], a1 = pv[1];
#pragma unroll
        for (int k = 1; k < 8; ++k) { a0 += pv[2 * k]; a1 += pv[2 * k + 1]; }
        float f[8] = {bflo(v[0]), bfhi(v[0]), bflo(v[1]), bfhi(v[1]), bflo(v[2]), bfhi(v[2]), bflo(v[3]), bfhi(v[3])};
#pragma unroll
        for (int j = 0; j < 4; ++j) { f[j] = bf16r(f[j] + bf16r(a0[j])); f[4 + j] = bf16r(f[4 + j] + bf16r(a1[j])); }
        const u32x4 hv = {pack2(f[0], f[1]), pack2(f[2], f[3]), pack2(f[4], f[5]), pack2(f[6], f[7])};
        st16_dev(wrs, OFF_H + ((size_t)row * 512 + lane * 8) * 2, hv);
        st16_dev(wrs, OFF_HN + ((size_t)((row >> 4) * 16 + (lane >> 2)) * 64 + (lane & 3) * 16 + (row & 15)) * 16, ln_row(f));
    }
    WSTAMP(7);
    arrive(cnt_ptr(ws, u, 3), wave);
    if (wg < 28) gemm_core<G_FFN1, TT / 2>(ws, lds, wrs, tt0 + (wg / 14) * (TT / 2), wg % 14, wave, [&] { wait_for<SL>(cnt_ptr(ws, u, 3), 32, wave, cap); WSTAMP(8); });
    WSTAMP(9);
    arrive(cnt_ptr(ws, u, 4), wave);
    gemm_core<G_DOWN, TT / 2>(ws, lds, wrs, tt0 + (wg >> 4) * (TT / 2), wg & 15, wave, [&] { wait_for<SL>(cnt_ptr(ws, u, 4), 32, wave, cap); WSTAMP(10); });
    WSTAMP(11);
    arrive(cnt_ptr(ws, u, 5), wave);
    wait_for<SL>(cnt_ptr(ws, u, 5), 32, wave, cap);
    WSTAMP(12);
    if (lrow < TT * 16) {
        const int lane = get_lane();
        const size_t i = (size_t)row * 512 + lane * 8;
        const unsigned po = (unsigned)(OFF_P2 + i * 4);
        f32x4 pv[8];
#pragma unroll
        for (int k = 0; k < 4; ++k) {
            pv[2 * k] = __builtin_bit_cast(f32x4, __builtin_amdgcn_raw_buffer_load_b128(wrs, po, k * S_ * D_ * 4, 16));
            pv[2 * k + 1] = __builtin_bit_cast(f32x4, __builtin_amdgcn_raw_buffer_load_b128(wrs, po + 16, k * S_ * D_ * 4, 16));
        }
        const u32x4 v = __builtin_amdgcn_raw_buffer_load_b128(wrs, (unsigned)(OFF_H + i * 2), 0, 16);
        __builtin_amdgcn_sched_barrier(0);
        const f32x4 a0 = (pv[0] + pv[2]) + (pv[4] + pv[6]), a1 = (pv[1] + pv[3]) + (pv[5] + pv[7]);
        const u32x4 o = {pack2(bflo(v[0]) + bf16r(a0[0]), bfhi(v[0]) + bf16r(a0[1])), pack2(bflo(v[1]) + bf16r(a0[2]), bfhi(v[1]) + bf16r(a0[3])),
                         pack2(bflo(v[2]) + bf16r(a1[0]), bfhi(v[2]) + bf16r(a1[1])), pack2(bflo(v[3]) + bf16r(a1[2]), bfhi(v[3]) + bf16r(a1[3]))};
        *(u32x4*)(out + i) = o;
    }
    WSTAMP(13);
}

__global__ void __launch_bounds__(256, 1) chain_kernel(const unsigned short* mem0, const unsigned short* x, unsigned short* out, char* ws) {
    __shared__ __attribute__((aligned(16))) char lds[CPH > 1 ? 128 * 1024 : 128 * 1024 + 24576];
    char* const l_mem = lds;
    char* const l_sx = l_mem + 8192;
    char* const l_ox = l_sx + 12288;
    char* const l_wo = l_ox + 4096;
    const int wave = __builtin_amdgcn_readfirstlane(threadIdx.x >> 6);
    const int lane = get_lane();
    const int tid = wave * 64 + lane;
    const __amdgpu_buffer_rsrc_t prs = __builtin_amdgcn_make_buffer_rsrc(ws + OFF_PART, 0, 1024 * 1024, 0x00020000);
    const __amdgpu_buffer_rsrc_t mrs = __builtin_amdgcn_make_buffer_rsrc(ws + OFF_MEMV, 0, 64 * 65536, 0x00020000);

    const __amdgpu_buffer_rsrc_t wrs = __builtin_amdgcn_make_buffer_rsrc(ws, 0, (unsigned)WS_END, 0x00020000);
#define ENDSTAMP() do { } while (0)
    if (blockIdx.x >= NCW + NRW) {
        const int wk = blockIdx.x - (NCW + NRW), grp = wk >> 5, wg = wk & 31;
        unsigned cap = SPIN_CAP;
#pragma unroll 1
        for (int i = 0; i < 8; ++i) {
            const int rb = 2 * i + (wk >> 6);
            gemm_core<G_KV, 8>(ws, lds, wrs, rb * 8, wk & 63, wave, [] {});
            arrive(cnt_ptr(ws, rb, 6), wave);
        }
#pragma unroll 1
        for (int u = grp; u < 13; u += 4) worker_unit<8, false>(x, out, ws, lds, wrs, u, 4 * u, wg, wave, cap);
        {
            const int u = 13 + ((grp + 3) & 3);
            worker_unit<4, false>(x, out, ws, lds, wrs, u, 52 + 2 * (u - 13), wg, wave, cap);
        }
        if (grp == 1 || grp == 2) worker_unit<2, false>(x, out, ws, lds, wrs, 16 + grp, 59 + grp, wg, wave, cap);
        ENDSTAMP();
        return;
    }
    unsigned gt0;
    {
        const unsigned toff = (unsigned)(OFF_CNT + 31 * 8 * 128);
        if (blockIdx.x == 0 && wave == 0)
            __builtin_amdgcn_raw_buffer_store_b32(((unsigned)__builtin_amdgcn_s_memrealtime() - 1000u) | 1u, wrs, lane == 0 ? toff : 0x40000000u, 0, 16);
        unsigned o = toff; gt0 = 0;
        for (unsigned spins = 0; spins <= SPIN_CAP && gt0 == 0; ++spins) {
            asm volatile("" : "+v"(o)::"memory");
            gt0 = (unsigned)__builtin_amdgcn_readfirstlane(__builtin_amdgcn_raw_buffer_load_b32(wrs, o, 0, 16));
        }
    }
    if (blockIdx.x >= NCW) {
        const int rw = blockIdx.x - NCW;
        unsigned cap = SPIN_CAP;
        if (wave < RWV) {
        const int r = rw * RWV + wave;
        const int t = r >> 2, gh = (r >> 1) & 1, rh = r & 1;
        const unsigned moff = (lane >> 3) < NCOPY ? (unsigned)((lane >> 3) * 8192 + (t >> 1) * 512 + (((t & 1) * 2 + gh) * 8 + 4 * rh) * 16 + (lane & 7) * 8) : 0x40000000u;
        for (int s = 0; s < NSEG_ - 1; ++s) {
            const unsigned tag = ((s >> 1) + 1) & 1;
            unsigned base = ((s & 1) * 128 + r) * 4096 + lane * 16;
            u32x4 x[4];
            if (s > 0) wait_grid(gt0, GE, GP + GE - GG - RLO, GP);
            for (unsigned spins = 0;; ++spins) {
                unsigned bad = 0;
                asm volatile("" : "+v"(base)::"memory");
#pragma unroll
                for (int k = 0; k < 4; ++k) x[k] = __builtin_amdgcn_raw_buffer_load_b128(prs, base + k * 1024, 0, 16);
                if (tag) {
                    const u32x4 a4 = (x[0] & x[1]) & (x[2] & x[3]);
                    bad = ~((a4[0] & a4[1]) & (a4[2] & a4[3]));
                } else {
                    const u32x4 o4 = (x[0] | x[1]) | (x[2] | x[3]);
                    bad = (o4[0] | o4[1]) | (o4[2] | o4[3]);
                }
                if (__all((bad & 1u) == 0u)) break;
                if (spins >= (unsigned)__builtin_amdgcn_readfirstlane(cap)) { cap = 0; break; }
            }
            STAMP(0);
            f32x4 a = (__builtin_bit_cast(f32x4, x[0]) + __builtin_bit_cast(f32x4, x[1])) + (__builtin_bit_cast(f32x4, x[2]) + __builtin_bit_cast(f32x4, x[3]));
#pragma unroll
            for (int i = 0; i < 4; ++i) a[i] = add_x32(add_x16(add_x8(a[i])));
            u32x2 o = {pack2(a[0], a[1]), pack2(a[2], a[3])};
            if (o[0] == 0xFFFFFFFFu) o[0] = 0x7FC07FC0u;
            if (o[1] == 0xFFFFFFFFu) o[1] = 0x7FC07FC0u;
            __builtin_amdgcn_raw_buffer_store_b64(o, mrs, (s + 1) * 65536 + moff, 0, 16);
            STAMP(1);
#pragma unroll
            for (int k = 0; k < 4; ++k) __builtin_amdgcn_raw_buffer_store_b128(x[k], prs, base + k * 1024, 0, 16);
        }
        }
        __syncthreads();
        if (rw < 64) worker_unit<2, true>(x, out, ws, lds, wrs, 19 + (rw >> 5), 62 + (rw >> 5), rw & 31, wave, cap);
        ENDSTAMP();
        return;
    }
    const int h = blockIdx.x & 31, part = blockIdx.x >> 5;
    const int c = lane & 15, g = lane >> 4;
    bf16x8 wq[2][16], wk[2][16], wv[2][16];
    {
        const char* wb = ws + OFF_WQKV + ((size_t)((h * 8 + wave * 2) * 16) * 64 + lane) * 16;
#pragma unroll
        for (int t = 0; t < 2; ++t)
#pragma unroll
            for (int ks = 0; ks < 16; ++ks) {
                wq[t][ks] = *(const bf16x8*)(wb + (size_t)(t * 16 + ks) * 1024);
                wk[t][ks] = *(const bf16x8*)(wb + 4 * MB + (size_t)(t * 16 + ks) * 1024);
                wv[t][ks] = *(const bf16x8*)(wb + 8 * MB + (size_t)(t * 16 + ks) * 1024);
            }
    }
    for (int i = wave; i < 128 / CPH; i += 4) {
        const int tile = part * (32 / CPH) + (i >> 2), kk = i & 3;
        *(u32x4*)(l_wo + i * 1024 + lane * 16) = *(const u32x4*)(ws + OFF_WO + ((size_t)(tile * 128 + h * 4 + kk) * 64 + lane) * 16);
    }
#pragma unroll
    for (int i = 0; i < 2; ++i) {
        const int idx = tid + i * 256;
        const int row = idx >> 6, ch = idx & 63;
        const u32x4 v = *(const u32x4*)(mem0 + row * 512 + ch * 8);
        *(u32x4*)(l_mem + (ch >> 2) * 512 + ((ch & 3) * 8 + row) * 16) = v;
    }
    const unsigned kfo = (unsigned)(OFF_KF + ((size_t)((h * 4 + wave) * 2) * 64 + lane) * 16);
#define LD_DEV(off) __builtin_bit_cast(bf16x8, __builtin_amdgcn_raw_buffer_load_b128(wrs, (off), 0, 16))
    const unsigned kmo = (unsigned)(OFF_KM + ((size_t)(h * 4 + wave) * 64 + lane) * 16);
    const unsigned vmo = (unsigned)(OFF_VM + ((size_t)((h * 4 + wave) * 2) * 64 + lane) * 8);
    const int mrd = (g * 8 + (c & 7)) * 16;
    const int srd = (c >> 3) * 1024 + (g * 16 + (c & 7)) * 16;
    const int srd2 = 2048 + ((g & 1) * 16 + (c & 7)) * 16 + (2 * (g >> 1) + (c >> 3)) * 4;
    const unsigned poff = c < 8 ? (unsigned)((((part * (32 / CPH) + wave * TPW) * 4 + (g >> 1) * 2 + (c >> 2)) * 32 + h) * 128 + ((c & 3) * 2 + (g & 1)) * 16) : 0x40000000u;
    unsigned cap = SPIN_CAP;
    wait_hint(wrs, (unsigned)(OFF_CNT + 6 * 128), 64, cap);
    bf16x8 kf0 = LD_DEV(kfo), kf1 = LD_DEV(kfo + 1024);
    bf16x8 vf0 = LD_DEV(kfo + 16 * MB), vf1 = LD_DEV(kfo + 16 * MB + 1024);
    __syncthreads();
    unsigned gdel = GDELAY;
#define VACC0(ks) (((ks) & 1) ? aw0 : av0)
#define VACC1(ks) (((ks) & 1) ? aw1 : av1)
#define VSTEP(ks) do { MFMA_VV(VACC0(ks), mf[(ks) & 7], wv[0][ks]); MFMA_VV(VACC1(ks), mf[(ks) & 7], wv[1][ks]); \
                       if ((ks) < 8) mf[(ks) & 7] = *(const bf16x8*)(l_mem + ((ks) + 8) * 512 + mrd); } while (0)
    for (int s = 0; s < NSEG_; ++s) {
        STAMP(0);
        FS(0);
        f32x4 aq0, aq1, ak0, ak1, av0, av1, aw0, aw1;
        bf16x8 mf[8];
#pragma unroll
        for (int ks = 0; ks < 8; ++ks) mf[ks] = *(const bf16x8*)(l_mem + ks * 512 + mrd);
#pragma unroll
        for (int ks = 0; ks < 16; ++ks) {
            const bf16x8 m = mf[ks & 7];
            if (ks == 0) { MFMA_AA0(aq0, wq[0][ks], m); MFMA_AA0(aq1, wq[1][ks], m); MFMA_AA0(ak0, wk[0][ks], m); MFMA_AA0(ak1, wk[1][ks], m); }
            else { MFMA_AA(aq0, wq[0][ks], m); MFMA_AA(aq1, wq[1][ks], m); MFMA_AA(ak0, wk[0][ks], m); MFMA_AA(ak1, wk[1][ks], m); }
            mf[ks & 7] = *(const bf16x8*)(l_mem + ((ks + 8) & 15) * 512 + mrd);
        }
        FS(1);
        MFMA_VV0(av0, mf[0], wv[0][0]); MFMA_VV0(av1, mf[0], wv[1][0]);
        mf[0] = *(const bf16x8*)(l_mem + 8 * 512 + mrd);
        asm volatile("" : "+v"(aq0), "+v"(aq1), "+v"(ak0), "+v"(ak1));
        bf16x8 qB = pack8(aq0, aq1);
        bf16x8 kA = pack8(ak0, ak1);
        NOP_IN2(qB, kA);
        f32x4 s0, s1, s2;
        MFMA_VV0(s0, kf0, qB); MFMA_VV0(s1, kf1, qB); MFMA_VV0(s2, kA, qB);
        MFMA_VV0(aw0, mf[1], wv[0][1]); MFMA_VV0(aw1, mf[1], wv[1][1]);
        mf[1] = *(const bf16x8*)(l_mem + 9 * 512 + mrd);
        asm volatile("" : "+v"(s0), "+v"(s1), "+v"(s2));
        *(f32x4*)(l_sx + (wave * 3 + 0) * 1024 + lane * 16) = s0;
        *(f32x4*)(l_sx + (wave * 3 + 1) * 1024 + lane * 16) = s1;
        *(f32x4*)(l_sx + (wave * 3 + 2) * 1024 + lane * 16) = s2;
        VSTEP(2);
        STAMP(1);
        FS(2);
        __syncthreads();
        FS(3);
        STAMP(2);
        f32x4 sa = *(const f32x4*)(l_sx + 0 * 3072 + srd), sb = *(const f32x4*)(l_sx + 1 * 3072 + srd);
        f32x4 sc = *(const f32x4*)(l_sx + 2 * 3072 + srd), sd = *(const f32x4*)(l_sx + 3 * 3072 + srd);
        float ma = *(const float*)(l_sx + 0 * 3072 + srd2), mb = *(const float*)(l_sx + 1 * 3072 + srd2);
        float mc = *(const float*)(l_sx + 2 * 3072 + srd2), md = *(const float*)(l_sx + 3 * 3072 + srd2);
        VSTEP(3); VSTEP(4); VSTEP(5); VSTEP(6); VSTEP(7); VSTEP(8); VSTEP(9);
        VSTEP(10); VSTEP(11); VSTEP(12); VSTEP(13); VSTEP(14); VSTEP(15);
#define MAX2(a, b) __builtin_amdgcn_fmed3f(a, b, __builtin_inff())
        __builtin_amdgcn_sched_barrier(0);
        FS(4);
        f32x4 e = (sa + sb) + (sc + sd);
        float em = (ma + mb) + (mc + md);
        unsigned r01 = pack2(e[0], e[1]), r23 = pack2(e[2], e[3]), rm = pack2(em, em);
        e[0] = bflo(r01) * SCALE_; e[1] = bfhi(r01) * SCALE_; e[2] = bflo(r23) * SCALE_; e[3] = bfhi(r23) * SCALE_; em = bflo(rm) * SCALE_;
        r01 = pack2(e[0], e[1]); r23 = pack2(e[2], e[3]); rm = pack2(em, em);
        e[0] = bflo(r01); e[1] = bfhi(r01); e[2] = bflo(r23); e[3] = bfhi(r23); em = bflo(rm);
        float mx = MAX2(MAX2(MAX2(e[0], e[1]), MAX2(e[2], e[3])), em);
        mx = MAX2(mx, __uint_as_float(ror8(__float_as_uint(mx))));
        { auto r = __builtin_amdgcn_permlane16_swap(__float_as_uint(mx), __float_as_uint(mx), false, false);
          mx = MAX2(__uint_as_float(r[0]), __uint_as_float(r[1])); }
        { auto r = __builtin_amdgcn_permlane32_swap(__float_as_uint(mx), __float_as_uint(mx), false, false);
          mx = MAX2(__uint_as_float(r[0]), __uint_as_float(r[1])); }
        e[0] = __builtin_amdgcn_exp2f((e[0] - mx) * LOG2E_); e[1] = __builtin_amdgcn_exp2f((e[1] - mx) * LOG2E_);
        e[2] = __builtin_amdgcn_exp2f((e[2] - mx) * LOG2E_); e[3] = __builtin_amdgcn_exp2f((e[3] - mx) * LOG2E_);
        em = __builtin_amdgcn_exp2f((em - mx) * LOG2E_);
        const float sum = add_x32(add_x16(add_x8(((e[0] + e[1]) + (e[2] + e[3])) + em)));
        const float inv = __builtin_amdgcn_rcpf(sum);
        e *= inv; em *= inv;
        FS(5);
        const unsigned p0 = pack2(e[0], e[1]), p1 = pack2(e[2], e[3]);
        const u32x4 pb0u = {p0, p1, ror8(p0), ror8(p1)};
        const unsigned dm = pack2(em, __uint_as_float(ror8(__float_as_uint(em))));
        auto sw = __builtin_amdgcn_permlane32_swap(dm, dm, false, false);
        const u32x2 pb1u = {g < 2 ? sw[0] : 0u, g < 2 ? sw[1] : 0u};
        bf16x8 pB0 = __builtin_bit_cast(bf16x8, pb0u);
        bf16x4 pB1 = __builtin_bit_cast(bf16x4, pb1u);
        NOP_OUT4(av0, av1, aw0, aw1);
        bf16x4 vA0 = pack4(av0 + aw0), vA1 = pack4(av1 + aw1);
        if (s == NSEG_ - 1) {
            if (part == 0) {
                __builtin_amdgcn_raw_buffer_store_b128(__builtin_bit_cast(u32x4, kA), wrs, kmo + s * 131072, 0, 16);
                __builtin_amdgcn_raw_buffer_store_b64(__builtin_bit_cast(u32x2, vA0), wrs, vmo + s * 131072, 0, 16);
                __builtin_amdgcn_raw_buffer_store_b64(__builtin_bit_cast(u32x2, vA1), wrs, vmo + s * 131072 + 512, 0, 16);
                arrive(cnt_ptr(ws, NUNIT - 1, 0), wave);
            }
            break;
        }
        FS(6);
        const char* wop = l_wo + wave * TPW * 4 * 1024 + lane * 16;
        constexpr int NM = TPW * 4;
        bf16x8 wf[8];
#pragma unroll
        for (int i = 0; i < 8; ++i) wf[i] = *(const bf16x8*)(wop + ((i % TPW) * 4 + (i / TPW)) * 1024);
        __builtin_amdgcn_sched_barrier(0);
        bf16x8 pB1x = ext8(pB1), vA0x = ext8(vA0), vA1x = ext8(vA1);
        asm volatile("s_nop 1" : "+v"(pB0), "+v"(pB1x), "+v"(vA0x), "+v"(vA1x));
        f32x4 o0, o1;
        MFMA_VV0(o0, vf0, pB0); MFMA_VV0(o1, vf1, pB0);
        MFMA_VV(o0, vA0x, pB1x); MFMA_VV(o1, vA1x, pB1x);
        NOP_OUT2(o0, o1);
        *(bf16x8*)(l_ox + wave * 1024 + lane * 16) = pack8(o0, o1);
        FS(7);
        __syncthreads();
        FS(8);
        STAMP(3);
        bf16x8 oB[4];
#pragma unroll
        for (int kk = 0; kk < 4; ++kk) oB[kk] = *(const bf16x8*)(l_ox + kk * 1024 + lane * 16);
        const unsigned tag = ((s >> 1) + 1) & 1;
        const unsigned pbase = (s & 1) * 128 * 4096 + poff;
        f32x4 pa[TPW];
#pragma unroll
        for (int i = 0; i < NM; ++i) {
            const int kk = i / TPW, t = i % TPW;
            const bf16x8 w = wf[i & 7];
            if (kk == 0) MFMA_VV0(pa[t], w, oB[kk]); else MFMA_VV(pa[t], w, oB[kk]);
            if (i + 8 < NM) { const int n = i + 8; wf[i & 7] = *(const bf16x8*)(wop + ((n % TPW) * 4 + (n / TPW)) * 1024); }
        }
        FS(9);
        NOP_OUT2(pa[TPW - 2], pa[TPW - 1]);
        {
            u32x4 pu[TPW];
#pragma unroll
            for (int q = 0; q < TPW; ++q) {
                asm volatile("" : "+v"(pa[q]));
                pu[q] = (__builtin_bit_cast(u32x4, pa[q]) & ~1u) | tag;
            }
#pragma unroll
            for (int q = 0; q < TPW; ++q) asm volatile("" : "+v"(pu[q]));
#pragma unroll
            for (int q = 0; q < TPW; ++q) __builtin_amdgcn_raw_buffer_store_b128(pu[q], prs, pbase, q * 4 * 4096, 16);
        }
        FS(10);
        if (part == 0) {
            __builtin_amdgcn_raw_buffer_store_b128(__builtin_bit_cast(u32x4, kA), wrs, kmo + s * 131072, 0, 16);
            __builtin_amdgcn_raw_buffer_store_b64(__builtin_bit_cast(u32x2, vA0), wrs, vmo + s * 131072, 0, 16);
            __builtin_amdgcn_raw_buffer_store_b64(__builtin_bit_cast(u32x2, vA1), wrs, vmo + s * 131072 + 512, 0, 16);
        }
        {
            if ((s & 3) == 3) wait_hint(wrs, (unsigned)(OFF_CNT + (((s + 1) >> 2) * 8 + 6) * 128), 64, cap);
            const unsigned so = kfo + (unsigned)(s + 1) * 32 * 8 * 1024;
            kf0 = LD_DEV(so); kf1 = LD_DEV(so + 1024);
            vf0 = LD_DEV(so + 16 * MB); vf1 = LD_DEV(so + 16 * MB + 1024);
        }
        STAMP(4);
        FS(11);
        wait_grid(gt0, GG, GG - GE - 8, GG + CHI);
        {
            unsigned off = (s + 1) * 65536 + (blockIdx.x % NCOPY) * 8192 + wave * 2048 + lane * 16;
            u32x4 y0, y1;
            for (unsigned spins = 0;; ++spins) {
                asm volatile("" : "+v"(off)::"memory");
                y0 = __builtin_amdgcn_raw_buffer_load_b128(mrs, off, 0, 16);
                y1 = __builtin_amdgcn_raw_buffer_load_b128(mrs, off + 1024, 0, 16);
                const unsigned mxu = max(max(max(y0[0], y0[1]), max(y0[2], y0[3])), max(max(y1[0], y1[1]), max(y1[2], y1[3])));
                const bool ok = mxu != 0xFFFFFFFFu;
                if (__all(ok)) {
                    break;
                }
                if (spins >= (unsigned)__builtin_amdgcn_readfirstlane(cap)) { cap = 0; break; }
            }
            STAMP(5);
            *(u32x4*)(l_mem + wave * 2048 + lane * 16) = y0;
            *(u32x4*)(l_mem + wave * 2048 + 1024 + lane * 16) = y1;
        }
        asm volatile("s_waitcnt vmcnt(0)" ::: "memory");
        __syncthreads();
        if (part == 0 && wave == 0 && get_lane() == 0) {
            const int u = unit_of_last_seg(s);
            if (u >= 0) __hip_atomic_fetch_add(cnt_ptr(ws, u, 0), 1u, __ATOMIC_RELAXED, __HIP_MEMORY_SCOPE_AGENT);
        }
    }
}

extern "C" void kernel_launch(void* const* d_in, const int* in_sizes, int n_in, void* d_out, int out_size, void* d_ws,
                              size_t ws_size, hipStream_t stream) {
    const unsigned short* x = (const unsigned short*)d_in[0];
    const unsigned short* mem0 = (const unsigned short*)d_in[1];
    const unsigned short* Wq = (const unsigned short*)d_in[2];
    const unsigned short* Wk = (const unsigned short*)d_in[3];
    const unsigned short* Wv = (const unsigned short*)d_in[4];
    const unsigned short* Wo = (const unsigned short*)d_in[5];
    const unsigned short* Wg = (const unsigned short*)d_in[8];
    const unsigned short* Wu = (const unsigned short*)d_in[9];
    const unsigned short* Wd = (const unsigned short*)d_in[10];
    char* ws = (char*)d_ws;
    prep_kernel<<<NB_PREP, 256, 0, stream>>>(x, Wq, Wk, Wv, Wo, Wg, Wu, Wd, ws);
    chain_kernel<<<NCW + NRW + NWK, 256, 0, stream>>>(mem0, x, (unsigned short*)d_out, ws);
}
```

```cpp
#include <hip/hip_runtime.h>
#include <hip/hip_bf16.h>
#include <stdint.h>
#include <type_traits>

constexpr int S_ = 2048, D_ = 512, H_ = 32, DH_ = 128, M_ = 8, SEG_ = 32, NSEG_ = 64, F_ = 1792;

typedef short bf16x8 __attribute__((ext_vector_type(8)));
typedef short bf16x4 __attribute__((ext_vector_type(4)));
typedef float f32x4 __attribute__((ext_vector_type(4)));
typedef unsigned u32x4 __attribute__((ext_vector_type(4)));
typedef unsigned u32x2 __attribute__((ext_vector_type(2)));
typedef __bf16 bfv2 __attribute__((ext_vector_type(2)));

__device__ __forceinline__ unsigned pack2(float a, float b) {
    bfv2 v = {(__bf16)a, (__bf16)b};
    return __builtin_bit_cast(unsigned, v);
}
__device__ __forceinline__ float bf16r(float a) { return (float)(__bf16)a; }
__device__ __forceinline__ float bflo(unsigned u) { return __uint_as_float(u << 16); }
__device__ __forceinline__ float bfhi(unsigned u) { return __uint_as_float(u & 0xffff0000u); }
__device__ __forceinline__ bf16x8 pack8(f32x4 a, f32x4 b) {
    u32x4 r = {pack2(a[0], a[1]), pack2(a[2], a[3]), pack2(b[0], b[1]), pack2(b[2], b[3])};
    return __builtin_bit_cast(bf16x8, r);
}
__device__ __forceinline__ bf16x4 pack4(f32x4 a) {
    u32x2 r = {pack2(a[0], a[1]), pack2(a[2], a[3])};
    return __builtin_bit_cast(bf16x4, r);
}
__device__ __forceinline__ int get_lane() {
    int l;
    asm volatile("v_mbcnt_lo_u32_b32 %0, -1, 0\n\tv_mbcnt_hi_u32_b32 %0, -1, %0" : "=v"(l));
    return l;
}
#define MFMA32(a, b, c) __builtin_amdgcn_mfma_f32_16x16x32_bf16(a, b, c, 0, 0, 0)
__device__ __forceinline__ bf16x8 ext8(bf16x4 a) { bf16x8 r = {a[0], a[1], a[2], a[3], 0, 0, 0, 0}; return r; }
#define MFMA16(a, b, c) MFMA32(ext8(a), ext8(b), c)

constexpr size_t MB = 1024 * 1024;
constexpr size_t OFF_WQKV = 0;
constexpr size_t OFF_WO = OFF_WQKV + 12 * MB;
constexpr size_t OFF_WGU = OFF_WO + 4 * MB;
constexpr size_t OFF_WD = OFF_WGU + 4 * MB;
constexpr size_t OFF_XN = OFF_WD + 2 * MB;
constexpr size_t OFF_QF = OFF_XN + 2 * MB;
constexpr size_t OFF_KF = OFF_QF + 16 * MB;
constexpr size_t OFF_VF = OFF_KF + 16 * MB;
constexpr size_t OFF_KM = OFF_VF + 16 * MB;
constexpr size_t OFF_VM = OFF_KM + 8 * MB;
constexpr size_t OFF_OF = OFF_VM + 8 * MB;
constexpr size_t OFF_P1 = OFF_OF + 16 * MB;
constexpr size_t OFF_H = OFF_P1 + 32 * MB;
constexpr size_t OFF_HN = OFF_H + 2 * MB;
constexpr size_t OFF_AF = OFF_HN + 2 * MB;
constexpr size_t OFF_P2 = OFF_AF + 8 * MB;
constexpr size_t OFF_PART = OFF_P2 + 16 * MB;
constexpr size_t OFF_MEMV = OFF_PART + 1 * MB;
constexpr size_t OFF_CNT = OFF_MEMV + 4 * MB;
constexpr size_t WS_END = OFF_CNT + 1 * MB;

__device__ __forceinline__ float wave_sum(float v) {
#pragma unroll
    for (int m = 1; m < 64; m <<= 1) v += __shfl_xor(v, m);
    return v;
}
__device__ __forceinline__ u32x4 ln_row(const float (&x)[8]) {
    float s = 0.f;
#pragma unroll
    for (int j = 0; j < 8; ++j) s += x[j];
    const float mean = wave_sum(s) * (1.0f / 512.0f);
    float q = 0.f;
#pragma unroll
    for (int j = 0; j < 8; ++j) { const float d = x[j] - mean; q += d * d; }
    const float var = wave_sum(q) * (1.0f / 512.0f);
    const float mb = bf16r(mean);
    const float vb = bf16r(var);
    const float den = bf16r(sqrtf(bf16r(vb + 1e-5f)));
    float y[8];
#pragma unroll
    for (int j = 0; j < 8; ++j) y[j] = bf16r(x[j] - mb) / den;
    u32x4 r = {pack2(y[0], y[1]), pack2(y[2], y[3]), pack2(y[4], y[5]), pack2(y[6], y[7])};
    return r;
}
__device__ __forceinline__ void store_row_frag(char* base, int row, int lane, u32x4 v) {
    const int tt = row >> 4, c = row & 15, ks = lane >> 2, g = lane & 3;
    *(u32x4*)(base + ((size_t)(tt * 16 + ks) * 64 + g * 16 + c) * 16) = v;
}

constexpr int NB_QKV = 0, NB_WO = 0, NB_GU = 0, NB_WD = 0, NB_LN = 512, NB_CLR = 64 + 256 + 8;
constexpr int NB_PREP = NB_QKV + NB_WO + NB_GU + NB_WD + NB_LN + NB_CLR;

__global__ void __launch_bounds__(256) prep_kernel(const unsigned short* __restrict__ x, const unsigned short* __restrict__ Wq,
                                                   const unsigned short* __restrict__ Wk, const unsigned short* __restrict__ Wv,
                                                   const unsigned short* __restrict__ Wo, const unsigned short* __restrict__ Wg,
                                                   const unsigned short* __restrict__ Wu, const unsigned short* __restrict__ Wd,
                                                   char* __restrict__ ws) {
    __shared__ __attribute__((aligned(16))) unsigned short t[32][264];
    int b = blockIdx.x;
    const int tid = threadIdx.x, wave = tid >> 6, lane = tid & 63;
    const unsigned short* src; int N, ks, nb, KS; bool perm = false; char* dst; int mode = 0;
    if (b < NB_QKV) {
        const int m = b >> 8; b &= 255;
        src = m == 0 ? Wq : (m == 1 ? Wk : Wv); N = 4096; KS = 16; ks = b & 15; nb = b >> 4; dst = ws + OFF_WQKV + (size_t)m * 4 * MB;
    } else if (b < NB_QKV + NB_WO) {
        b -= NB_QKV; src = Wo; N = 512; KS = 128; ks = b & 127; nb = b >> 7; perm = true; dst = ws + OFF_WO;
    } else if (b < NB_QKV + NB_WO + NB_GU) {
        b -= NB_QKV + NB_WO; const int up = b >= 112; if (up) b -= 112;
        src = up ? Wu : Wg; N = 1792; KS = 16; ks = b & 15; nb = b >> 4; dst = ws + OFF_WGU; mode = 1 + up;
    } else if (b < NB_QKV + NB_WO + NB_GU + NB_WD) {
        b -= NB_QKV + NB_WO + NB_GU; src = Wd; N = 512; KS = 56; ks = b % 56; nb = b / 56; perm = true; dst = ws + OFF_WD;
    } else if (b < NB_QKV + NB_WO + NB_GU + NB_WD + NB_LN) {
        b -= NB_QKV + NB_WO + NB_GU + NB_WD;
        const int row = b * 4 + wave;
        const u32x4 v = *(const u32x4*)(x + (size_t)row * 512 + lane * 8);
        float f[8] = {bflo(v[0]), bfhi(v[0]), bflo(v[1]), bfhi(v[1]), bflo(v[2]), bfhi(v[2]), bflo(v[3]), bfhi(v[3])};
        store_row_frag(ws + OFF_XN, row, lane, ln_row(f));
        return;
    } else {
        b -= NB_QKV + NB_WO + NB_GU + NB_WD + NB_LN;
        const bool p = b < 64 || b >= 320;
        char* d = b < 64 ? ws + OFF_PART + (size_t)b * 16384 : ws + OFF_MEMV + (size_t)(b - 64) * 16384;
        const unsigned f = p ? 0u : 0xFFFFFFFFu;
        const u32x4 v = {f, f, f, f};
#pragma unroll
        for (int i = 0; i < 4; ++i) *(u32x4*)(d + i * 4096 + tid * 16) = v;
        return;
    }
    {
        u32x4 v[4];
#pragma unroll
        for (int i = 0; i < 4; ++i) v[i] = *(const u32x4*)(src + (size_t)(ks * 32 + i * 8 + (tid >> 5)) * N + nb * 256 + (tid & 31) * 8);
#pragma unroll
        for (int i = 0; i < 4; ++i) *(u32x4*)&t[i * 8 + (tid >> 5)][(tid & 31) * 8] = v[i];
    }
    __syncthreads();
    const int c = lane & 15, g = lane >> 4;
#pragma unroll
    for (int q = 0; q < 4; ++q) {
        unsigned short e[8];
#pragma unroll
        for (int j = 0; j < 8; ++j) {
            const int k = perm ? (j < 4 ? 4 * g + j : 16 + 4 * g + j - 4) : 8 * g + j;
            e[j] = t[k][(q * 4 + wave) * 16 + c];
        }
        int nt = nb * 16 + q * 4 + wave;
        if (mode) nt = (nt >> 1) * 4 + (mode == 2 ? 2 : 0) + (nt & 1);
        u32x4 o = {e[0] | ((unsigned)e[1] << 16), e[2] | ((unsigned)e[3] << 16), e[4] | ((unsigned)e[5] << 16), e[6] | ((unsigned)e[7] << 16)};
        *(u32x4*)(dst + ((size_t)(nt * KS + ks) * 64 + lane) * 16) = o;
    }
}

#define TR_READ(p) __builtin_amdgcn_ds_read_tr16_b64_v4i16((__attribute__((address_space(3))) bf16x4*)(p))
template <int B0, int B1>
__device__ __forceinline__ void frags_issue(const unsigned short* src, bf16x8 (&f)[2][16]) {
    const int lane = get_lane();
    const unsigned short* sp = src + (size_t)(lane >> 2) * 4096 + (lane & 3) * 8;
#pragma unroll
    for (int b = B0; b < B1; ++b)
#pragma unroll
        for (int i = 0; i < 4; ++i) f[i & 1][2 * b + (i >> 1)] = *(const bf16x8*)(sp + (size_t)(b * 64 + i * 16) * 4096);
    __builtin_amdgcn_sched_barrier(0);
}
template <bool PIN_A, int B0, int B1>
__device__ __forceinline__ void frags_transpose(char* scr, bf16x8 (&f)[2][16]) {
    const int lane = get_lane();
    const int c = lane & 15, g = lane >> 4;
#pragma unroll
    for (int b = B0; b < B1; ++b) {
#pragma unroll
        for (int i = 0; i < 4; ++i) *(bf16x8*)(scr + (i * 16 + (lane >> 2)) * 80 + (lane & 3) * 16) = f[i & 1][2 * b + (i >> 1)];
        asm volatile("s_waitcnt lgkmcnt(0)" ::: "memory");
#pragma unroll
        for (int kk = 0; kk < 2; ++kk)
#pragma unroll
            for (int t = 0; t < 2; ++t) {
                const char* ap = scr + (kk * 32 + 8 * g + (c >> 2)) * 80 + (t * 16 + 4 * (c & 3)) * 2;
                const bf16x4 lo4 = TR_READ(ap), hi4 = TR_READ(ap + 4 * 80);
                bf16x8 r = {lo4[0], lo4[1], lo4[2], lo4[3], hi4[0], hi4[1], hi4[2], hi4[3]};
                if constexpr (PIN_A) asm volatile("" : "+a"(r)); else asm volatile("" : "+v"(r));
                f[t][2 * b + kk] = r;
            }
        __builtin_amdgcn_sched_barrier(0);
        asm volatile("s_waitcnt lgkmcnt(0)" ::: "memory");
    }
}

enum { G_KV = 0, G_OUTP = 1, G_FFN1 = 2, G_DOWN = 3, G_Q = 4 };

__device__ __forceinline__ void st16_dev(const __amdgpu_buffer_rsrc_t rs, size_t off, u32x4 v) {
    __builtin_amdgcn_raw_buffer_store_b128(v, rs, (unsigned)off, 0, 16);
}

template <int MODE, int TT, class SYNC>
__device__ __forceinline__ void gemm_core(char* ws, char* xl, const __amdgpu_buffer_rsrc_t wrs, int tt0, int cb, int wave, SYNC sync) {
    int lane;
    constexpr int NKS = MODE == G_DOWN ? 14 : 16;
    constexpr int KST = MODE == G_OUTP ? 128 : (MODE == G_DOWN ? 56 : 16);
    constexpr int NT = MODE == G_FFN1 ? 4 : 2;
    constexpr int NIT = 1;
    lane = get_lane();
    int ks0 = 0, cg = cb;
    const char* xsrc; unsigned wsrc;
    if (MODE == G_KV || MODE == G_Q) { xsrc = ws + OFF_XN; wsrc = (unsigned)OFF_WQKV; }
    else if (MODE == G_OUTP) { xsrc = ws + OFF_OF; wsrc = (unsigned)OFF_WO; ks0 = (cb & 7) * 16; cg = cb >> 3; }
    else if (MODE == G_FFN1) { xsrc = ws + OFF_HN; wsrc = (unsigned)OFF_WGU; }
    else { xsrc = ws + OFF_AF; wsrc = (unsigned)OFF_WD; ks0 = (cb & 3) * 14; cg = cb >> 2; }
    const int c = lane & 15, g = lane >> 4;
    auto tile0 = [&](int it) -> int {
        if (MODE == G_KV) { const int ci = 32 + cg; return (ci >> 5) * 256 + (ci & 31) * 8 + wave * 2; }
        else if (MODE == G_Q) return cg * 8 + wave * 2;
        else if (MODE == G_FFN1) return (cg * 4 + wave) * 4;
        else return cg * 8 + wave * 2;
    };
    constexpr int PD = NKS / 2;
    bf16x8 wr[PD][NT];
    const unsigned wvo = wsrc + (unsigned)((tile0(0) * KST + ks0) * 64 + lane) * 16;
#define LD_W(t, ks) __builtin_bit_cast(bf16x8, __builtin_amdgcn_raw_buffer_load_b128(wrs, wvo, ((t) * KST + (ks)) * 1024, 16))
#pragma unroll
    for (int ks = 0; ks < PD; ++ks)
#pragma unroll
        for (int t = 0; t < NT; ++t) wr[ks][t] = LD_W(t, ks);
    sync();
#pragma unroll
    for (int j = 0; j < (TT * NKS + 3) / 4; ++j) {
        const int i = j * 4 + wave;
        if (i < TT * NKS) {
            const int tt = i / NKS, ks = i % NKS;
            if constexpr (MODE == G_KV || MODE == G_Q)
                __builtin_amdgcn_global_load_lds((const unsigned*)(xsrc + ((size_t)((tt0 + tt) * KST + ks0 + ks) * 64 + lane) * 16),
                                                 (unsigned*)(xl + (size_t)i * 1024 + lane * 16), 16, 0, 0);
            else
                __builtin_amdgcn_global_load_lds((const unsigned*)(xsrc + ((size_t)((tt0 + tt) * KST + ks0 + ks) * 64 + lane) * 16),
                                                 (unsigned*)(xl + (size_t)i * 1024 + lane * 16), 16, 0, 16);
        }
    }
    asm volatile("s_waitcnt vmcnt(0)" ::: "memory");
    __syncthreads();
#pragma unroll 1
    for (int it = 0; it < NIT; ++it) {
        asm volatile("" ::: "memory");
        const int nt0 = tile0(it);
        int mat = 0, head = 0;
        if (MODE == G_KV) { const int ci = 32 + cg; mat = ci >> 5; head = ci & 31; }
        f32x4 acc[NT][TT];
#pragma unroll
        for (int t = 0; t < NT; ++t)
#pragma unroll
            for (int tt = 0; tt < TT; ++tt) acc[t][tt] = (f32x4){0.f, 0.f, 0.f, 0.f};
        const bool vmat = (MODE == G_KV) && (mat == 2);
        auto kloop = [&](auto swapped) {
            bf16x8 xf[2][TT];
#pragma unroll
            for (int tt = 0; tt < TT; ++tt) xf[0][tt] = *(const bf16x8*)(xl + (tt * NKS) * 1024 + lane * 16);
#pragma unroll
            for (int ks = 0; ks < NKS; ++ks) {
                bf16x8 wf[NT];
#pragma unroll
                for (int t = 0; t < NT; ++t) {
                    wf[t] = wr[ks % PD][t];
                    if (ks + PD < NKS) wr[ks % PD][t] = LD_W(t, ks + PD);
                }
                if (ks + 1 < NKS) {
#pragma unroll
                    for (int tt = 0; tt < TT; ++tt) xf[(ks + 1) & 1][tt] = *(const bf16x8*)(xl + (tt * NKS + ks + 1) * 1024 + lane * 16);
                }
                __builtin_amdgcn_sched_barrier(0);
#pragma unroll
                for (int tt = 0; tt < TT; ++tt)
#pragma unroll
                    for (int t = 0; t < NT; ++t) {
                        if constexpr (decltype(swapped)::value) acc[t][tt] = MFMA32(xf[ks & 1][tt], wf[t], acc[t][tt]);
                        else acc[t][tt] = MFMA32(wf[t], xf[ks & 1][tt], acc[t][tt]);
                    }
                __builtin_amdgcn_sched_barrier(0);
            }
        };
        if (vmat) kloop(std::true_type{}); else kloop(std::false_type{});
        if (MODE == G_KV) {
            if (mat == 2) {
#pragma unroll
                for (int sg = 0; sg < TT / 2; ++sg)
#pragma unroll
                    for (int t = 0; t < 2; ++t) {
                        const int seg = tt0 / 2 + sg;
                        st16_dev(wrs, OFF_VF + ((size_t)(((seg * 32 + head) * 4 + wave) * 2 + t) * 64 + lane) * 16, __builtin_bit_cast(u32x4, pack8(acc[t][2 * sg], acc[t][2 * sg + 1])));
                    }
            } else {
#pragma unroll
                for (int tt = 0; tt < TT; ++tt) {
                    const int seg = tt0 / 2 + (tt >> 1);
                    st16_dev(wrs, OFF_KF + ((size_t)(((seg * 32 + head) * 4 + wave) * 2 + (tt & 1)) * 64 + lane) * 16, __builtin_bit_cast(u32x4, pack8(acc[0][tt], acc[1][tt])));
                }
            }
        } else if (MODE == G_Q) {
            __syncthreads();
#pragma unroll
            for (int tt = 0; tt < TT; ++tt) *(bf16x8*)(xl + (tt * 4 + wave) * 1024 + lane * 16) = pack8(acc[0][tt], acc[1][tt]);
        } else if (MODE == G_OUTP || MODE == G_DOWN) {
            const size_t dst = (MODE == G_OUTP ? OFF_P1 : OFF_P2) + (size_t)(MODE == G_OUTP ? (cb & 7) : (cb & 3)) * S_ * D_ * 4;
#pragma unroll
            for (int t = 0; t < 2; ++t)
#pragma unroll
                for (int tt = 0; tt < TT; ++tt)
                    st16_dev(wrs, dst + ((size_t)((tt0 + tt) * 16 + c) * D_ + (nt0 + t) * 16 + g * 4) * 4, __builtin_bit_cast(u32x4, acc[t][tt]));
        } else {
            const int ag = cg * 4 + wave;
#pragma unroll
            for (int tt = 0; tt < TT; ++tt) {
                f32x4 a0, a1;
#pragma unroll
                for (int i = 0; i < 4; ++i) {
                    a0[i] = bf16r(acc[0][tt][i]) * bf16r(acc[2][tt][i]);
                    a1[i] = bf16r(acc[1][tt][i]) * bf16r(acc[NT - 1][tt][i]);
                }
                st16_dev(wrs, OFF_AF + ((size_t)((tt0 + tt) * 56 + ag) * 64 + lane) * 16, __builtin_bit_cast(u32x4, pack8(a0, a1)));
            }
        }
    }
}

constexpr float SCALE_ = 0.08838834764831845f;
constexpr float LOG2E_ = 1.4426950408889634f;
constexpr unsigned SPIN_CAP = 1u << 17;

#define PH(i) do { } while (0)
#define FS(i) do { } while (0)
#define STAMP(i) do { } while (0)
__device__ __forceinline__ float max_x16(float v) {
    auto r = __builtin_amdgcn_permlane16_swap(__float_as_uint(v), __float_as_uint(v), false, false);
    return fmaxf(__uint_as_float(r[0]), __uint_as_float(r[1]));
}
__device__ __forceinline__ float max_x32(float v) {
    auto r = __builtin_amdgcn_permlane32_swap(__float_as_uint(v), __float_as_uint(v), false, false);
    return fmaxf(__uint_as_float(r[0]), __uint_as_float(r[1]));
}
__device__ __forceinline__ float add_x16(float v) {
    auto r = __builtin_amdgcn_permlane16_swap(__float_as_uint(v), __float_as_uint(v), false, false);
    return __uint_as_float(r[0]) + __uint_as_float(r[1]);
}
__device__ __forceinline__ float add_x32(float v) {
    auto r = __builtin_amdgcn_permlane32_swap(__float_as_uint(v), __float_as_uint(v), false, false);
    return __uint_as_float(r[0]) + __uint_as_float(r[1]);
}
__device__ __forceinline__ float add_x8(float v) {
    return v + __uint_as_float(__builtin_amdgcn_update_dpp(0u, __float_as_uint(v), 0x128, 0xf, 0xf, false));
}
#define MFMA_AA(acc, wa, b) asm volatile("v_mfma_f32_16x16x32_bf16 %0, %1, %2, %0" : "+v"(acc) : "a"(wa), "v"(b))
#define MFMA_AA0(acc, wa, b) asm volatile("v_mfma_f32_16x16x32_bf16 %0, %1, %2, 0" : "=&v"(acc) : "a"(wa), "v"(b))
#define MFMA_VV(acc, a, b) asm volatile("v_mfma_f32_16x16x32_bf16 %0, %1, %2, %0" : "+v"(acc) : "v"(a), "v"(b))
#define MFMA_VV0(acc, a, b) asm volatile("v_mfma_f32_16x16x32_bf16 %0, %1, %2, 0" : "=&v"(acc) : "v"(a), "v"(b))
#define NOP_IN2(a, b) asm volatile("s_nop 1" : "+v"(a), "+v"(b))
#define NOP_OUT2(a, b) asm volatile("s_nop 7\n\ts_nop 3" : "+v"(a), "+v"(b))
#define NOP_OUT4(a, b, c, d) asm volatile("s_nop 7\n\ts_nop 3" : "+v"(a), "+v"(b), "+v"(c), "+v"(d))

#define CPH 2
constexpr int NCW = 32 * CPH;
constexpr int TPW = 8 / CPH;
__device__ __forceinline__ float max_x8(float v) {
    return fmaxf(v, __uint_as_float(__builtin_amdgcn_update_dpp(0u, __float_as_uint(v), 0x128, 0xf, 0xf, false)));
}
__device__ __forceinline__ unsigned ror8(unsigned v) { return __builtin_amdgcn_update_dpp(0u, v, 0x128, 0xf, 0xf, false); }

#define HINTS 0
#define HPOS 2
#define DROPL 1
#define GDELAY 0
#define H2POS 1
#define HDELAY 0
#define NCOPY 8
#define GDEC 1
#define GINC 15
#define GMIN 60
#define SLBIG 32
#define SLCH 127
#define GP 352
#define GE 25
#define GG 105
#define RLO 20
#define CHI 60
#define GK 2
#define SLMID 20
#define GPM 340
#define GT1 (20 * GP)
#define GT2 (GT1 + 40 * GPM)
#define RWV 2
constexpr int NRW = 128 / RWV;
constexpr int NWK = 128;
constexpr int NUNIT = 21;
__device__ __forceinline__ int unit_of_last_seg(int s) {
    if (s < 52) return (s & 3) == 3 ? (s >> 2) : -1;
    if (s < 60) return (s & 1) ? 13 + ((s - 52) >> 1) : -1;
    return 17 + (s - 60);
}
constexpr unsigned HINT1 = (unsigned)(OFF_CNT + 32768), HINT2 = (unsigned)(OFF_CNT + 32768 + 16384);
__device__ __forceinline__ void wait_hint(const __amdgpu_buffer_rsrc_t rs, unsigned off, unsigned want, unsigned cap) {
    cap = (unsigned)__builtin_amdgcn_readfirstlane(cap);
    for (unsigned spins = 0; spins <= cap; ++spins) {
        asm volatile("" : "+v"(off)::"memory");
        const unsigned v = __builtin_amdgcn_raw_buffer_load_b32(rs, off, 0, 16);
        if ((unsigned)__builtin_amdgcn_readfirstlane(v) >= want) break;
    }
}
__device__ __forceinline__ void wait_grid(unsigned t0, unsigned off, int lo, int hi, bool addp) {
    const unsigned t = (unsigned)__builtin_amdgcn_s_memrealtime() - t0;
    unsigned d, l;
    if (t >= GT1 && t < GT2) {
        constexpr unsigned DL = GPM / GK;
        l = lo + (addp ? GPM : 0);
        d = l + DL - 1 - (t - GT1 + l + 8 * GPM + DL - 1 - off) % DL;
        l = hi + (addp ? GPM : 0);
    } else {
        constexpr unsigned DL = GP / GK;
        l = lo + (addp ? GP : 0);
        d = l + DL - 1 - (t - (t >= GT2 ? GT2 : 0) + l + 8 * GP + DL - 1 - off) % DL;
        l = hi + (addp ? GP : 0);
    }
    if (d <= l) {
        const unsigned target = t + d;
        while ((unsigned)__builtin_amdgcn_s_memrealtime() - t0 < target) { }
    }
}
typedef __attribute__((address_space(1))) unsigned gu32;
__device__ __forceinline__ gu32* cnt_ptr(char* ws, int u, int ph) { return (gu32*)(ws + OFF_CNT + (size_t)(u * 8 + ph) * 128); }
__device__ __forceinline__ void arrive(gu32* c, int wave) {
    asm volatile("s_waitcnt vmcnt(0)" ::: "memory");
    __syncthreads();
    if (wave == 0 && get_lane() == 0) __hip_atomic_fetch_add(c, 1u, __ATOMIC_RELAXED, __HIP_MEMORY_SCOPE_AGENT);
}
template <int SLP>
__device__ __forceinline__ void wait_for(gu32* c, unsigned target, int wave, unsigned& cap) {
    if (wave == 0) {
        for (unsigned spins = 0;; ++spins) {
            if ((unsigned)__builtin_amdgcn_readfirstlane(__hip_atomic_load(c, __ATOMIC_RELAXED, __HIP_MEMORY_SCOPE_AGENT)) >= target) break;
            if (spins >= cap) { cap = 0; break; }
            __builtin_amdgcn_s_sleep(SLP);
        }
    }
    asm volatile("" ::: "memory");
    __syncthreads();
}

__device__ __forceinline__ unsigned flag_off(int u, int ph, int wg) { return (unsigned)(OFF_CNT + 65536 + (size_t)(((u & 1) * 8 + ph) * 32 + wg) * 128); }
__device__ __forceinline__ void arrive_f(const __amdgpu_buffer_rsrc_t wrs, int u, int ph, int wg, int wave) {
    asm volatile("s_waitcnt vmcnt(0)" ::: "memory");
    __syncthreads();
    if (wave == 0) __builtin_amdgcn_raw_buffer_store_b32(1u, wrs, get_lane() == 0 ? flag_off(u, ph, wg) : 0x40000000u, 0, 16);
}
__device__ __forceinline__ void wait_f(const __amdgpu_buffer_rsrc_t wrs, int u, int ph, int wave, unsigned& cap) {
    if (wave == 0) {
        unsigned off = flag_off(u, ph, get_lane() & 31);
        for (unsigned spins = 0;; ++spins) {
            asm volatile("" : "+v"(off)::"memory");
            const unsigned v = __builtin_amdgcn_raw_buffer_load_b32(wrs, off, 0, 16);
            if (__all(v != 0u)) break;
            if (spins >= (unsigned)__builtin_amdgcn_readfirstlane(cap)) { cap = 0; break; }
        }
    }
    asm volatile("" ::: "memory");
    __syncthreads();
}

#define LD16_DEV(off) __builtin_bit_cast(bf16x8, __builtin_amdgcn_raw_buffer_load_b128(wrs, (unsigned)(off), 0, 16))
#define LD8_DEV(off) __builtin_bit_cast(bf16x4, __builtin_amdgcn_raw_buffer_load_b64(wrs, (unsigned)(off), 0, 16))
__device__ __forceinline__ void seg_attention(const char* qf, const __amdgpu_buffer_rsrc_t wrs, int seg, int h,
                                              const bf16x8 (&kf)[8], const bf16x8 (&vf)[8]) {
    const int lane = get_lane();
    const int c = lane & 15, g = lane >> 4;
    bf16x8 km[4]; bf16x4 vm[8];
#pragma unroll
    for (int w = 0; w < 4; ++w) km[w] = LD16_DEV(OFF_KM + ((size_t)((seg * 32 + h) * 4 + w) * 64 + lane) * 16);
#pragma unroll
    for (int i = 0; i < 8; ++i) vm[i] = LD8_DEV(OFF_VM + ((size_t)((seg * 32 + h) * 8 + i) * 64 + lane) * 8);
    __builtin_amdgcn_sched_barrier(0);
    const f32x4 z = {0, 0, 0, 0};
    f32x4 sc[2][3];
#pragma unroll
    for (int qt = 0; qt < 2; ++qt)
#pragma unroll
        for (int kt = 0; kt < 3; ++kt) sc[qt][kt] = z;
    bf16x8 q[2][4];
#pragma unroll
    for (int w = 0; w < 4; ++w) {
        q[0][w] = *(const bf16x8*)(qf + (0 * 4 + w) * 1024 + lane * 16);
        q[1][w] = *(const bf16x8*)(qf + (1 * 4 + w) * 1024 + lane * 16);
        sc[0][0] = MFMA32(kf[w * 2], q[0][w], sc[0][0]); sc[0][1] = MFMA32(kf[w * 2 + 1], q[0][w], sc[0][1]);
        sc[1][0] = MFMA32(kf[w * 2], q[1][w], sc[1][0]); sc[1][1] = MFMA32(kf[w * 2 + 1], q[1][w], sc[1][1]);
    }
#pragma unroll
    for (int w = 0; w < 4; ++w) { sc[0][2] = MFMA32(km[w], q[0][w], sc[0][2]); sc[1][2] = MFMA32(km[w], q[1][w], sc[1][2]); }
    bf16x8 pB0[2]; bf16x4 pB1[2];
#pragma unroll
    for (int qt = 0; qt < 2; ++qt) {
        const int qi = qt * 16 + c;
        float mx = -3.0e38f;
#pragma unroll
        for (int kt = 0; kt < 3; ++kt)
#pragma unroll
            for (int i = 0; i < 4; ++i) {
                float v = bf16r(bf16r(sc[qt][kt][i]) * SCALE_);
                const bool valid = kt == 2 ? (g < 2) : (kt * 16 + 4 * g + i <= qi);
                v = valid ? v : -3.0e38f;
                sc[qt][kt][i] = v;
                mx = fmaxf(mx, v);
            }
        mx = max_x32(max_x16(mx));
        float sum = 0.f;
#pragma unroll
        for (int kt = 0; kt < 3; ++kt)
#pragma unroll
            for (int i = 0; i < 4; ++i) {
                const bool valid = kt == 2 ? (g < 2) : (kt * 16 + 4 * g + i <= qi);
                const float e = valid ? __builtin_amdgcn_exp2f((sc[qt][kt][i] - mx) * LOG2E_) : 0.f;
                sc[qt][kt][i] = e;
                sum += e;
            }
        sum = add_x32(add_x16(sum));
        const float inv = __builtin_amdgcn_rcpf(sum);
#pragma unroll
        for (int kt = 0; kt < 3; ++kt) sc[qt][kt] *= inv;
        pB0[qt] = pack8(sc[qt][0], sc[qt][1]);
        pB1[qt] = pack4(sc[qt][2]);
    }
#pragma unroll
    for (int w = 0; w < 4; ++w) {
#pragma unroll
        for (int qt = 0; qt < 2; ++qt) {
            f32x4 o0 = MFMA32(vf[w * 2], pB0[qt], z);
            f32x4 o1 = MFMA32(vf[w * 2 + 1], pB0[qt], z);
            o0 = MFMA16(vm[w * 2], pB1[qt], o0);
            o1 = MFMA16(vm[w * 2 + 1], pB1[qt], o1);
            st16_dev(wrs, OFF_OF + ((size_t)((seg * 2 + qt) * 128 + h * 4 + w) * 64 + lane) * 16, __builtin_bit_cast(u32x4, pack8(o0, o1)));
        }
    }
}

__device__ __forceinline__ void kv_phase(const unsigned short* Wk, const unsigned short* Wv, char* ws, char* xl, const __amdgpu_buffer_rsrc_t wrs,
                                         int wk, int wave) {
    const int ci = wk & 63, vmat = ci >> 5, head = ci & 31;
    bf16x8 wr[2][16];
    frags_issue<0, 8>((vmat ? Wv : Wk) + head * 128 + wave * 32, wr);
    frags_transpose<false, 0, 8>(xl + wave * 8192, wr);
#pragma unroll 1
    for (int i = 0; i < 8; ++i) {
        const int rb = 2 * i + (wk >> 6);
        const int lane = get_lane();
        __syncthreads();
#pragma unroll
        for (int j = 0; j < 32; ++j) {
            const int q = j * 4 + wave;
            __builtin_amdgcn_global_load_lds((const unsigned*)(ws + OFF_XN + ((size_t)(rb * 128 + q) * 64 + lane) * 16),
                                             (unsigned*)(xl + (size_t)q * 1024 + lane * 16), 16, 0, 0);
        }
        asm volatile("s_waitcnt vmcnt(0)" ::: "memory");
        __syncthreads();
        f32x4 acc[2][8];
#pragma unroll
        for (int t = 0; t < 2; ++t)
#pragma unroll
            for (int tt = 0; tt < 8; ++tt) acc[t][tt] = (f32x4){0.f, 0.f, 0.f, 0.f};
        auto kloop = [&](auto swapped) {
            bf16x8 xf[2][8];
#pragma unroll
            for (int tt = 0; tt < 8; ++tt) xf[0][tt] = *(const bf16x8*)(xl + (tt * 16) * 1024 + lane * 16);
#pragma unroll
            for (int ks = 0; ks < 16; ++ks) {
                if (ks + 1 < 16) {
#pragma unroll
                    for (int tt = 0; tt < 8; ++tt) xf[(ks + 1) & 1][tt] = *(const bf16x8*)(xl + (tt * 16 + ks + 1) * 1024 + lane * 16);
                }
                __builtin_amdgcn_sched_barrier(0);
#pragma unroll
                for (int tt = 0; tt < 8; ++tt)
#pragma unroll
                    for (int t = 0; t < 2; ++t) {
                        if constexpr (decltype(swapped)::value) acc[t][tt] = MFMA32(xf[ks & 1][tt], wr[t][ks], acc[t][tt]);
                        else acc[t][tt] = MFMA32(wr[t][ks], xf[ks & 1][tt], acc[t][tt]);
                    }
                __builtin_amdgcn_sched_barrier(0);
            }
        };
        if (vmat) {
            kloop(std::true_type{});
#pragma unroll
            for (int sg = 0; sg < 4; ++sg)
#pragma unroll
                for (int t = 0; t < 2; ++t)
                    st16_dev(wrs, OFF_VF + ((size_t)((((rb * 4 + sg) * 32 + head) * 4 + wave) * 2 + t) * 64 + lane) * 16, __builtin_bit_cast(u32x4, pack8(acc[t][2 * sg], acc[t][2 * sg + 1])));
        } else {
            kloop(std::false_type{});
#pragma unroll
            for (int tt = 0; tt < 8; ++tt)
                st16_dev(wrs, OFF_KF + ((size_t)((((rb * 4 + (tt >> 1)) * 32 + head) * 4 + wave) * 2 + (tt & 1)) * 64 + lane) * 16, __builtin_bit_cast(u32x4, pack8(acc[0][tt], acc[1][tt])));
        }
        arrive(cnt_ptr(ws, rb, 6), wave);
    }
}

#define WSTAMP(i) do { } while (0)
template <int TT, bool FAST>
__device__ __forceinline__ void worker_unit(const unsigned short* x, unsigned short* out, char* ws, char* lds, const __amdgpu_buffer_rsrc_t wrs,
                                            int u, int seg0, int wg, int wave, unsigned& cap) {
    constexpr int SL = FAST ? 2 : (TT > 4 ? SLBIG : SLMID);
    const int tt0 = seg0 * 2;
#define ARR(ph) do { if constexpr (FAST) arrive_f(wrs, u, ph, wg, wave); else arrive(cnt_ptr(ws, u, ph), wave); } while (0)
#define WAI(ph) do { if constexpr (FAST) wait_f(wrs, u, ph, wave, cap); else wait_for<SL>(cnt_ptr(ws, u, ph), 32, wave, cap); } while (0)
    WSTAMP(0);
    wait_for<16>(cnt_ptr(ws, 30, 0), NRW * (4 - RWV), wave, cap);
    gemm_core<G_Q, TT>(ws, lds, wrs, tt0, wg, wave, [] {});
    WSTAMP(1);
    {
        wait_for<16>(cnt_ptr(ws, 30, 2), NRW * (4 - RWV), wave, cap);
        wait_for<16>(cnt_ptr(ws, seg0 >> 2, 6), 64, wave, cap);
        bf16x8 kf[8], vf[8];
        const int sw = wave < TT / 2 ? wave : 0;
        {
            const int lane = get_lane();
            const unsigned fb = (unsigned)(OFF_KF + ((size_t)((seg0 + sw) * 32 + wg) * 8 * 64 + lane) * 16);
#pragma unroll
            for (int i = 0; i < 8; ++i) { kf[i] = LD16_DEV(fb + i * 1024); vf[i] = LD16_DEV(fb + 16 * MB + i * 1024); }
        }
        wait_for<(FAST ? 2 : (TT > 4 ? SLCH : 32))>(cnt_ptr(ws, u, 0), 32, wave, cap);
        WSTAMP(2);
        if (wave < TT / 2) seg_attention(lds + wave * 8192, wrs, seg0 + wave, wg, kf, vf);
    }
    WSTAMP(3);
    ARR(1);
    gemm_core<G_OUTP, TT>(ws, lds, wrs, tt0, wg, wave, [&] { WAI(1); WSTAMP(4); });
    WSTAMP(5);
    ARR(2);
    WAI(2);
    WSTAMP(6);
    const int lrow = wg * 4 + wave;
    const int row = tt0 * 16 + lrow;
    if (lrow < TT * 16) {
        const int lane = get_lane();
        const unsigned po = (unsigned)(OFF_P1 + ((size_t)row * 512 + lane * 8) * 4);
        f32x4 pv[16];
#pragma unroll
        for (int k = 0; k < 8; ++k) {
            pv[2 * k] = __builtin_bit_cast(f32x4, __builtin_amdgcn_raw_buffer_load_b128(wrs, po, k * S_ * D_ * 4, 16));
            pv[2 * k + 1] = __builtin_bit_cast(f32x4, __builtin_amdgcn_raw_buffer_load_b128(wrs, po + 16, k * S_ * D_ * 4, 16));
        }
        const u32x4 v = *(const u32x4*)(x + (size_t)row * 512 + lane * 8);
        __builtin_amdgcn_sched_barrier(0);
        f32x4 a0 = pv[0], a1 = pv[1];
#pragma unroll
        for (int k = 1; k < 8; ++k) { a0 += pv[2 * k]; a1 += pv[2 * k + 1]; }
        float f[8] = {bflo(v[0]), bfhi(v[0]), bflo(v[1]), bfhi(v[1]), bflo(v[2]), bfhi(v[2]), bflo(v[3]), bfhi(v[3])};
#pragma unroll
        for (int j = 0; j < 4; ++j) { f[j] = bf16r(f[j] + bf16r(a0[j])); f[4 + j] = bf16r(f[4 + j] + bf16r(a1[j])); }
        const u32x4 hv = {pack2(f[0], f[1]), pack2(f[2], f[3]), pack2(f[4], f[5]), pack2(f[6], f[7])};
        st16_dev(wrs, OFF_H + ((size_t)row * 512 + lane * 8) * 2, hv);
        st16_dev(wrs, OFF_HN + ((size_t)((row >> 4) * 16 + (lane >> 2)) * 64 + (lane & 3) * 16 + (row & 15)) * 16, ln_row(f));
    }
    WSTAMP(7);
    ARR(3);
    if (wg < 28) gemm_core<G_FFN1, TT / 2>(ws, lds, wrs, tt0 + (wg / 14) * (TT / 2), wg % 14, wave, [&] { WAI(3); WSTAMP(8); });
    WSTAMP(9);
    ARR(4);
    gemm_core<G_DOWN, TT / 2>(ws, lds, wrs, tt0 + (wg >> 4) * (TT / 2), wg & 15, wave, [&] { WAI(4); WSTAMP(10); });
    WSTAMP(11);
    ARR(5);
    WAI(5);
    WSTAMP(12);
    if (lrow < TT * 16) {
        const int lane = get_lane();
        const size_t i = (size_t)row * 512 + lane * 8;
        const unsigned po = (unsigned)(OFF_P2 + i * 4);
        f32x4 pv[8];
#pragma unroll
        for (int k = 0; k < 4; ++k) {
            pv[2 * k] = __builtin_bit_cast(f32x4, __builtin_amdgcn_raw_buffer_load_b128(wrs, po, k * S_ * D_ * 4, 16));
            pv[2 * k + 1] = __builtin_bit_cast(f32x4, __builtin_amdgcn_raw_buffer_load_b128(wrs, po + 16, k * S_ * D_ * 4, 16));
        }
        const u32x4 v = __builtin_amdgcn_raw_buffer_load_b128(wrs, (unsigned)(OFF_H + i * 2), 0, 16);
        __builtin_amdgcn_sched_barrier(0);
        const f32x4 a0 = (pv[0] + pv[2]) + (pv[4] + pv[6]), a1 = (pv[1] + pv[3]) + (pv[5] + pv[7]);
        const u32x4 o = {pack2(bflo(v[0]) + bf16r(a0[0]), bfhi(v[0]) + bf16r(a0[1])), pack2(bflo(v[1]) + bf16r(a0[2]), bfhi(v[1]) + bf16r(a0[3])),
                         pack2(bflo(v[2]) + bf16r(a1[0]), bfhi(v[2]) + bf16r(a1[1])), pack2(bflo(v[3]) + bf16r(a1[2]), bfhi(v[3]) + bf16r(a1[3]))};
        *(u32x4*)(out + i) = o;
    }
    WSTAMP(13);
}

__global__ void __launch_bounds__(256, 1) chain_kernel(const unsigned short* mem0, const unsigned short* x, unsigned short* out, char* ws,
                                                      const unsigned short* Wg, const unsigned short* Wu, const unsigned short* Wd,
                                                      const unsigned short* Wq, const unsigned short* Wk, const unsigned short* Wv, const unsigned short* Wo) {
    __shared__ __attribute__((aligned(16))) char lds[CPH > 1 ? 128 * 1024 : 128 * 1024 + 24576];
    char* const l_mem = lds;
    char* const l_sx = l_mem + 8192;
    char* const l_ox = l_sx + 12288;
    char* const l_wo = l_ox + 4096;
    const int wave = __builtin_amdgcn_readfirstlane(threadIdx.x >> 6);
    const int lane = get_lane();
    const int tid = wave * 64 + lane;
    const __amdgpu_buffer_rsrc_t prs = __builtin_amdgcn_make_buffer_rsrc(ws + OFF_PART, 0, 1024 * 1024, 0x00020000);
    const __amdgpu_buffer_rsrc_t mrs = __builtin_amdgcn_make_buffer_rsrc(ws + OFF_MEMV, 0, 64 * 65536, 0x00020000);

    const __amdgpu_buffer_rsrc_t wrs = __builtin_amdgcn_make_buffer_rsrc(ws, 0, (unsigned)WS_END, 0x00020000);
#define ENDSTAMP() do { } while (0)
    if (blockIdx.x >= NCW + NRW) {
        const int wk = blockIdx.x - (NCW + NRW), grp = wk >> 5, wg = wk & 31;
        unsigned cap = SPIN_CAP;
        kv_phase(Wk, Wv, ws, lds, wrs, wk, wave);
#pragma unroll 1
        for (int u = grp; u < 13; u += 4) worker_unit<8, false>(x, out, ws, lds, wrs, u, 4 * u, wg, wave, cap);
        {
            const int u = 13 + ((grp + 3) & 3);
            worker_unit<4, false>(x, out, ws, lds, wrs, u, 52 + 2 * (u - 13), wg, wave, cap);
        }
        if (grp == 1 || grp == 2) worker_unit<2, false>(x, out, ws, lds, wrs, 16 + grp, 59 + grp, wg, wave, cap);
        ENDSTAMP();
        return;
    }
    unsigned gt0;
    {
        const unsigned toff = (unsigned)(OFF_CNT + 31 * 8 * 128);
        if (blockIdx.x == 0 && wave == 0)
            __builtin_amdgcn_raw_buffer_store_b32(((unsigned)__builtin_amdgcn_s_memrealtime() - 1000u) | 1u, wrs, lane == 0 ? toff : 0x40000000u, 0, 16);
        unsigned o = toff; gt0 = 0;
        for (unsigned spins = 0; spins <= SPIN_CAP && gt0 == 0; ++spins) {
            asm volatile("" : "+v"(o)::"memory");
            gt0 = (unsigned)__builtin_amdgcn_readfirstlane(__builtin_amdgcn_raw_buffer_load_b32(wrs, o, 0, 16));
        }
    }
    if (blockIdx.x >= NCW) {
        const int rw = blockIdx.x - NCW;
        unsigned cap = SPIN_CAP;
        if (wave < RWV) {
        const int r = rw * RWV + wave;
        const int t = r >> 2, gh = (r >> 1) & 1, rh = r & 1;
        const unsigned moff = (lane >> 3) < NCOPY ? (unsigned)((lane >> 3) * 8192 + (t >> 1) * 512 + (((t & 1) * 2 + gh) * 8 + 4 * rh) * 16 + (lane & 7) * 8) : 0x40000000u;
        for (int s = 0; s < NSEG_ - 1; ++s) {
            const unsigned tag = ((s >> 1) + 1) & 1;
            unsigned base = ((s & 1) * 128 + r) * 4096 + lane * 16;
            u32x4 x[4];
            if (s > 0) wait_grid(gt0, GE, GE - GG - RLO, 0, true);
            for (unsigned spins = 0;; ++spins) {
                unsigned bad = 0;
                asm volatile("" : "+v"(base)::"memory");
#pragma unroll
                for (int k = 0; k < 4; ++k) x[k] = __builtin_amdgcn_raw_buffer_load_b128(prs, base + k * 1024, 0, 16);
                if (tag) {
                    const u32x4 a4 = (x[0] & x[1]) & (x[2] & x[3]);
                    bad = ~((a4[0] & a4[1]) & (a4[2] & a4[3]));
                } else {
                    const u32x4 o4 = (x[0] | x[1]) | (x[2] | x[3]);
                    bad = (o4[0] | o4[1]) | (o4[2] | o4[3]);
                }
                if (__all((bad & 1u) == 0u)) break;
                if (spins >= (unsigned)__builtin_amdgcn_readfirstlane(cap)) { cap = 0; break; }
            }
            STAMP(0);
            f32x4 a = (__builtin_bit_cast(f32x4, x[0]) + __builtin_bit_cast(f32x4, x[1])) + (__builtin_bit_cast(f32x4, x[2]) + __builtin_bit_cast(f32x4, x[3]));
#pragma unroll
            for (int i = 0; i < 4; ++i) a[i] = add_x32(add_x16(add_x8(a[i])));
            u32x2 o = {pack2(a[0], a[1]), pack2(a[2], a[3])};
            if (o[0] == 0xFFFFFFFFu) o[0] = 0x7FC07FC0u;
            if (o[1] == 0xFFFFFFFFu) o[1] = 0x7FC07FC0u;
            __builtin_amdgcn_raw_buffer_store_b64(o, mrs, (s + 1) * 65536 + moff, 0, 16);
            STAMP(1);
            PH(0);
#pragma unroll
            for (int k = 0; k < 4; ++k) __builtin_amdgcn_raw_buffer_store_b128(x[k], prs, base + k * 1024, 0, 16);
        }
        } else {
            unsigned short (*t)[72] = (unsigned short (*)[72])(lds + wave * 8192);
            const int c = lane & 15, g = lane >> 4;
#pragma unroll 1
            for (int ph = 0; ph < 3; ++ph) {
                const int nblk = ph == 2 ? 336 : 256;
#pragma unroll 1
                for (int b = rw * (4 - RWV) + (wave - RWV); b < nblk; b += NRW * (4 - RWV)) {
                    const unsigned short* src; int N, KS, ks, nb, mode; unsigned dst; bool perm = false;
                    if (ph == 0) { src = Wq; N = 4096; KS = 16; ks = b & 15; nb = b >> 4; dst = (unsigned)OFF_WQKV; mode = 0; }
                    else if (ph == 1) { src = Wo; N = 512; KS = 128; ks = b & 127; nb = b >> 7; dst = (unsigned)OFF_WO; mode = 0; perm = true; }
                    else if (b < 224) { const int up = b >= 112, bb = up ? b - 112 : b; src = up ? Wu : Wg; N = 1792; KS = 16; ks = bb & 15; nb = bb >> 4; dst = (unsigned)OFF_WGU; mode = 1 + up; }
                    else { const int bb = b - 224; src = Wd; N = 512; KS = 56; ks = bb % 56; nb = bb / 56; dst = (unsigned)OFF_WD; mode = 0; perm = true; }
                    u32x4 v[4][4];
#pragma unroll
                    for (int sb = 0; sb < 4; ++sb)
#pragma unroll
                        for (int i = 0; i < 4; ++i) v[sb][i] = *(const u32x4*)(src + (size_t)(ks * 32 + i * 8 + (lane >> 3)) * N + nb * 256 + sb * 64 + (lane & 7) * 8);
                    __builtin_amdgcn_sched_barrier(0);
#pragma unroll
                    for (int sb = 0; sb < 4; ++sb) {
#pragma unroll
                        for (int i = 0; i < 4; ++i) *(u32x4*)&t[i * 8 + (lane >> 3)][(lane & 7) * 8] = v[sb][i];
                        asm volatile("s_waitcnt lgkmcnt(0)" ::: "memory");
#pragma unroll
                        for (int q = 0; q < 4; ++q) {
                            const int r0 = (perm ? 4 * g : 8 * g) + (c >> 2), r1 = r0 + (perm ? 16 : 4);
                            const bf16x4 lo4 = TR_READ(&t[r0][q * 16 + 4 * (c & 3)]), hi4 = TR_READ(&t[r1][q * 16 + 4 * (c & 3)]);
                            const bf16x8 o = {lo4[0], lo4[1], lo4[2], lo4[3], hi4[0], hi4[1], hi4[2], hi4[3]};
                            int nt = nb * 16 + sb * 4 + q;
                            if (mode) nt = (nt >> 1) * 4 + (mode == 2 ? 2 : 0) + (nt & 1);
                            st16_dev(wrs, dst + (unsigned)((nt * KS + ks) * 64 + lane) * 16, __builtin_bit_cast(u32x4, o));
                        }
                        asm volatile("s_waitcnt lgkmcnt(0)" ::: "memory");
                    }
                }
                asm volatile("s_waitcnt vmcnt(0)" ::: "memory");
                if (lane == 0) __hip_atomic_fetch_add(cnt_ptr(ws, 30, ph), 1u, __ATOMIC_RELAXED, __HIP_MEMORY_SCOPE_AGENT);
            }
        }
        __syncthreads();
        if (rw < 64) worker_unit<2, true>(x, out, ws, lds, wrs, 19 + (rw >> 5), 62 + (rw >> 5), rw & 31, wave, cap);
        ENDSTAMP();
        return;
    }
    const int h = blockIdx.x & 31, part = blockIdx.x >> 5;
    const int c = lane & 15, g = lane >> 4;
    bf16x8 wq[2][16], wk[2][16], wv[2][16];
    {
        char* scr = lds + 96 * 1024 + wave * 8192;
        unsigned short (*t)[72] = (unsigned short (*)[72])scr;
        u32x4 v[8 / CPH][4];
#pragma unroll
        for (int nb = 0; nb < 8 / CPH; ++nb)
#pragma unroll
            for (int i = 0; i < 4; ++i) v[nb][i] = *(const u32x4*)(Wo + (size_t)(h * 128 + wave * 32 + i * 8 + (lane >> 3)) * 512 + part * (512 / CPH) + nb * 64 + (lane & 7) * 8);
        frags_issue<0, 8>(Wq + h * 128 + wave * 32, wq);
#pragma unroll
        for (int nb = 0; nb < 8 / CPH; ++nb) {
#pragma unroll
            for (int i = 0; i < 4; ++i) *(u32x4*)&t[i * 8 + (lane >> 3)][(lane & 7) * 8] = v[nb][i];
            asm volatile("s_waitcnt lgkmcnt(0)" ::: "memory");
#pragma unroll
            for (int q = 0; q < 4; ++q) {
                const bf16x4 lo4 = TR_READ(&t[4 * g + (c >> 2)][q * 16 + 4 * (c & 3)]), hi4 = TR_READ(&t[16 + 4 * g + (c >> 2)][q * 16 + 4 * (c & 3)]);
                const bf16x8 o = {lo4[0], lo4[1], lo4[2], lo4[3], hi4[0], hi4[1], hi4[2], hi4[3]};
                *(bf16x8*)(l_wo + ((nb * 4 + q) * 4 + wave) * 1024 + lane * 16) = o;
            }
            asm volatile("s_waitcnt lgkmcnt(0)" ::: "memory");
        }
        __builtin_amdgcn_sched_barrier(0);
        frags_issue<0, 4>(Wk + h * 128 + wave * 32, wk);
        frags_transpose<true, 0, 8>(scr, wq);
        frags_issue<4, 8>(Wk + h * 128 + wave * 32, wk);
        frags_issue<0, 4>(Wv + h * 128 + wave * 32, wv);
        frags_transpose<true, 0, 4>(scr, wk);
        frags_issue<4, 8>(Wv + h * 128 + wave * 32, wv);
        frags_transpose<true, 4, 8>(scr, wk);
        frags_transpose<false, 0, 8>(scr, wv);
    }
#pragma unroll
    for (int i = 0; i < 2; ++i) {
        const int idx = tid + i * 256;
        const int row = idx >> 6, ch = idx & 63;
        const u32x4 v = *(const u32x4*)(mem0 + row * 512 + ch * 8);
        *(u32x4*)(l_mem + (ch >> 2) * 512 + ((ch & 3) * 8 + row) * 16) = v;
    }
    const unsigned kfo = (unsigned)(OFF_KF + ((size_t)((h * 4 + wave) * 2) * 64 + lane) * 16);
#define LD_DEV(off) __builtin_bit_cast(bf16x8, __builtin_amdgcn_raw_buffer_load_b128(wrs, (off), 0, 16))
    const unsigned kmo = (unsigned)(OFF_KM + ((size_t)(h * 4 + wave) * 64 + lane) * 16);
    const unsigned vmo = (unsigned)(OFF_VM + ((size_t)((h * 4 + wave) * 2) * 64 + lane) * 8);
    const int mrd = (g * 8 + (c & 7)) * 16;
    const int srd = (c >> 3) * 1024 + (g * 16 + (c & 7)) * 16;
    const int srd2 = 2048 + ((g & 1) * 16 + (c & 7)) * 16 + (2 * (g >> 1) + (c >> 3)) * 4;
    const unsigned poff = c < 8 ? (unsigned)((((part * (32 / CPH) + wave * TPW) * 4 + (g >> 1) * 2 + (c >> 2)) * 32 + h) * 128 + ((c & 3) * 2 + (g & 1)) * 16) : 0x40000000u;
    unsigned cap = SPIN_CAP;
    wait_hint(wrs, (unsigned)(OFF_CNT + 6 * 128), 64, cap);
    bf16x8 kf0 = LD_DEV(kfo), kf1 = LD_DEV(kfo + 1024);
    bf16x8 vf0 = LD_DEV(kfo + 16 * MB), vf1 = LD_DEV(kfo + 16 * MB + 1024);
    __syncthreads();
    unsigned gdel = GDELAY;
    unsigned kvv = 0;
#define VACC0(ks) (((ks) & 1) ? aw0 : av0)
#define VACC1(ks) (((ks) & 1) ? aw1 : av1)
#define VSTEP(ks) do { MFMA_VV(VACC0(ks), mf[(ks) & 7], wv[0][ks]); MFMA_VV(VACC1(ks), mf[(ks) & 7], wv[1][ks]); \
                       if ((ks) < 8) mf[(ks) & 7] = *(const bf16x8*)(l_mem + ((ks) + 8) * 512 + mrd); } while (0)
    for (int s = 0; s < NSEG_; ++s) {
        STAMP(0);
        FS(0);
        f32x4 aq0, aq1, ak0, ak1, av0, av1, aw0, aw1;
        bf16x8 mf[8];
#pragma unroll
        for (int ks = 0; ks < 8; ++ks) mf[ks] = *(const bf16x8*)(l_mem + ks * 512 + mrd);
#pragma unroll
        for (int ks = 0; ks < 16; ++ks) {
            const bf16x8 m = mf[ks & 7];
            if (ks == 0) { MFMA_AA0(aq0, wq[0][ks], m); MFMA_AA0(aq1, wq[1][ks], m); MFMA_AA0(ak0, wk[0][ks], m); MFMA_AA0(ak1, wk[1][ks], m); }
            else { MFMA_AA(aq0, wq[0][ks], m); MFMA_AA(aq1, wq[1][ks], m); MFMA_AA(ak0, wk[0][ks], m); MFMA_AA(ak1, wk[1][ks], m); }
            mf[ks & 7] = *(const bf16x8*)(l_mem + ((ks + 8) & 15) * 512 + mrd);
        }
        FS(1);
        MFMA_VV0(av0, mf[0], wv[0][0]); MFMA_VV0(av1, mf[0], wv[1][0]);
        mf[0] = *(const bf16x8*)(l_mem + 8 * 512 + mrd);
        asm volatile("" : "+v"(aq0), "+v"(aq1), "+v"(ak0), "+v"(ak1));
        bf16x8 qB = pack8(aq0, aq1);
        bf16x8 kA = pack8(ak0, ak1);
        NOP_IN2(qB, kA);
        f32x4 s0, s1, s2;
        MFMA_VV0(s0, kf0, qB); MFMA_VV0(s1, kf1, qB); MFMA_VV0(s2, kA, qB);
        MFMA_VV0(aw0, mf[1], wv[0][1]); MFMA_VV0(aw1, mf[1], wv[1][1]);
        mf[1] = *(const bf16x8*)(l_mem + 9 * 512 + mrd);
        asm volatile("" : "+v"(s0), "+v"(s1), "+v"(s2));
        *(f32x4*)(l_sx + (wave * 3 + 0) * 1024 + lane * 16) = s0;
        *(f32x4*)(l_sx + (wave * 3 + 1) * 1024 + lane * 16) = s1;
        *(f32x4*)(l_sx + (wave * 3 + 2) * 1024 + lane * 16) = s2;
        VSTEP(2);
        STAMP(1);
        FS(2);
        __syncthreads();
        FS(3);
        STAMP(2);
        f32x4 sa = *(const f32x4*)(l_sx + 0 * 3072 + srd), sb = *(const f32x4*)(l_sx + 1 * 3072 + srd);
        f32x4 sc = *(const f32x4*)(l_sx + 2 * 3072 + srd), sd = *(const f32x4*)(l_sx + 3 * 3072 + srd);
        float ma = *(const float*)(l_sx + 0 * 3072 + srd2), mb = *(const float*)(l_sx + 1 * 3072 + srd2);
        float mc = *(const float*)(l_sx + 2 * 3072 + srd2), md = *(const float*)(l_sx + 3 * 3072 + srd2);
        VSTEP(3); VSTEP(4); VSTEP(5); VSTEP(6); VSTEP(7); VSTEP(8); VSTEP(9);
        VSTEP(10); VSTEP(11); VSTEP(12); VSTEP(13); VSTEP(14); VSTEP(15);
#define MAX2(a, b) __builtin_amdgcn_fmed3f(a, b, __builtin_inff())
        __builtin_amdgcn_sched_barrier(0);
        FS(4);
        f32x4 e = (sa + sb) + (sc + sd);
        float em = (ma + mb) + (mc + md);
        unsigned r01 = pack2(e[0], e[1]), r23 = pack2(e[2], e[3]), rm = pack2(em, em);
        e[0] = bflo(r01) * SCALE_; e[1] = bfhi(r01) * SCALE_; e[2] = bflo(r23) * SCALE_; e[3] = bfhi(r23) * SCALE_; em = bflo(rm) * SCALE_;
        r01 = pack2(e[0], e[1]); r23 = pack2(e[2], e[3]); rm = pack2(em, em);
        e[0] = bflo(r01); e[1] = bfhi(r01); e[2] = bflo(r23); e[3] = bfhi(r23); em = bflo(rm);
        float mx = MAX2(MAX2(MAX2(e[0], e[1]), MAX2(e[2], e[3])), em);
        mx = MAX2(mx, __uint_as_float(ror8(__float_as_uint(mx))));
        { auto r = __builtin_amdgcn_permlane16_swap(__float_as_uint(mx), __float_as_uint(mx), false, false);
          mx = MAX2(__uint_as_float(r[0]), __uint_as_float(r[1])); }
        { auto r = __builtin_amdgcn_permlane32_swap(__float_as_uint(mx), __float_as_uint(mx), false, false);
          mx = MAX2(__uint_as_float(r[0]), __uint_as_float(r[1])); }
        e[0] = __builtin_amdgcn_exp2f((e[0] - mx) * LOG2E_); e[1] = __builtin_amdgcn_exp2f((e[1] - mx) * LOG2E_);
        e[2] = __builtin_amdgcn_exp2f((e[2] - mx) * LOG2E_); e[3] = __builtin_amdgcn_exp2f((e[3] - mx) * LOG2E_);
        em = __builtin_amdgcn_exp2f((em - mx) * LOG2E_);
        const float sum = add_x32(add_x16(add_x8(((e[0] + e[1]) + (e[2] + e[3])) + em)));
        const float inv = __builtin_amdgcn_rcpf(sum);
        e *= inv; em *= inv;
        FS(5);
        const unsigned p0 = pack2(e[0], e[1]), p1 = pack2(e[2], e[3]);
        const u32x4 pb0u = {p0, p1, ror8(p0), ror8(p1)};
        const unsigned dm = pack2(em, __uint_as_float(ror8(__float_as_uint(em))));
        auto sw = __builtin_amdgcn_permlane32_swap(dm, dm, false, false);
        const u32x2 pb1u = {g < 2 ? sw[0] : 0u, g < 2 ? sw[1] : 0u};
        bf16x8 pB0 = __builtin_bit_cast(bf16x8, pb0u);
        bf16x4 pB1 = __builtin_bit_cast(bf16x4, pb1u);
        NOP_OUT4(av0, av1, aw0, aw1);
        bf16x4 vA0 = pack4(av0 + aw0), vA1 = pack4(av1 + aw1);
        if (s == NSEG_ - 1) {
            if (part == 0) {
                __builtin_amdgcn_raw_buffer_store_b128(__builtin_bit_cast(u32x4, kA), wrs, kmo + s * 131072, 0, 16);
                __builtin_amdgcn_raw_buffer_store_b64(__builtin_bit_cast(u32x2, vA0), wrs, vmo + s * 131072, 0, 16);
                __builtin_amdgcn_raw_buffer_store_b64(__builtin_bit_cast(u32x2, vA1), wrs, vmo + s * 131072 + 512, 0, 16);
                arrive(cnt_ptr(ws, NUNIT - 1, 0), wave);
            }
            break;
        }
        FS(6);
        const char* wop = l_wo + wave * TPW * 4 * 1024 + lane * 16;
        constexpr int NM = TPW * 4;
        bf16x8 wf[8];
#pragma unroll
        for (int i = 0; i < 8; ++i) wf[i] = *(const bf16x8*)(wop + ((i % TPW) * 4 + (i / TPW)) * 1024);
        __builtin_amdgcn_sched_barrier(0);
        bf16x8 pB1x = ext8(pB1), vA0x = ext8(vA0), vA1x = ext8(vA1);
        asm volatile("s_nop 1" : "+v"(pB0), "+v"(pB1x), "+v"(vA0x), "+v"(vA1x));
        f32x4 o0, o1;
        MFMA_VV0(o0, vf0, pB0); MFMA_VV0(o1, vf1, pB0);
        MFMA_VV(o0, vA0x, pB1x); MFMA_VV(o1, vA1x, pB1x);
        NOP_OUT2(o0, o1);
        *(bf16x8*)(l_ox + wave * 1024 + lane * 16) = pack8(o0, o1);
        FS(7);
        __syncthreads();
        FS(8);
        STAMP(3);
        bf16x8 oB[4];
#pragma unroll
        for (int kk = 0; kk < 4; ++kk) oB[kk] = *(const bf16x8*)(l_ox + kk * 1024 + lane * 16);
        const unsigned tag = ((s >> 1) + 1) & 1;
        const unsigned pbase = (s & 1) * 128 * 4096 + poff;
        f32x4 pa[TPW];
#pragma unroll
        for (int i = 0; i < NM; ++i) {
            const int kk = i / TPW, t = i % TPW;
            const bf16x8 w = wf[i & 7];
            if (kk == 0) MFMA_VV0(pa[t], w, oB[kk]); else MFMA_VV(pa[t], w, oB[kk]);
            if (i + 8 < NM) { const int n = i + 8; wf[i & 7] = *(const bf16x8*)(wop + ((n % TPW) * 4 + (n / TPW)) * 1024); }
        }
        FS(9);
        NOP_OUT2(pa[TPW - 2], pa[TPW - 1]);
        {
            u32x4 pu[TPW];
#pragma unroll
            for (int q = 0; q < TPW; ++q) {
                asm volatile("" : "+v"(pa[q]));
                pu[q] = (__builtin_bit_cast(u32x4, pa[q]) & ~1u) | tag;
            }
#pragma unroll
            for (int q = 0; q < TPW; ++q) asm volatile("" : "+v"(pu[q]));
#pragma unroll
            for (int q = 0; q < TPW; ++q) __builtin_amdgcn_raw_buffer_store_b128(pu[q], prs, pbase, q * 4 * 4096, 16);
        }
        FS(10);
        PH(0);
        if (part == 0) {
            __builtin_amdgcn_raw_buffer_store_b128(__builtin_bit_cast(u32x4, kA), wrs, kmo + s * 131072, 0, 16);
            __builtin_amdgcn_raw_buffer_store_b64(__builtin_bit_cast(u32x2, vA0), wrs, vmo + s * 131072, 0, 16);
            __builtin_amdgcn_raw_buffer_store_b64(__builtin_bit_cast(u32x2, vA1), wrs, vmo + s * 131072 + 512, 0, 16);
        }
        {
            if ((s & 3) == 3 && (unsigned)__builtin_amdgcn_readfirstlane(kvv) < 64u)
                wait_hint(wrs, (unsigned)(OFF_CNT + (((s + 1) >> 2) * 8 + 6) * 128), 64, cap);
            if ((s & 3) == 2) kvv = __builtin_amdgcn_raw_buffer_load_b32(wrs, (unsigned)(OFF_CNT + (((s + 2) >> 2) * 8 + 6) * 128), 0, 16);
            const unsigned so = kfo + (unsigned)(s + 1) * 32 * 8 * 1024;
            kf0 = LD_DEV(so); kf1 = LD_DEV(so + 1024);
            vf0 = LD_DEV(so + 16 * MB); vf1 = LD_DEV(so + 16 * MB + 1024);
        }
        STAMP(4);
        FS(11);
        wait_grid(gt0, GG, GG - GE - 8, GG + CHI, false);
        {
            unsigned off = (s + 1) * 65536 + (blockIdx.x % NCOPY) * 8192 + wave * 2048 + lane * 16;
            u32x4 y0, y1;
            for (unsigned spins = 0;; ++spins) {
                asm volatile("" : "+v"(off)::"memory");
                y0 = __builtin_amdgcn_raw_buffer_load_b128(mrs, off, 0, 16);
                y1 = __builtin_amdgcn_raw_buffer_load_b128(mrs, off + 1024, 0, 16);
                const unsigned mxu = max(max(max(y0[0], y0[1]), max(y0[2], y0[3])), max(max(y1[0], y1[1]), max(y1[2], y1[3])));
                const bool ok = mxu != 0xFFFFFFFFu;
                if (__all(ok)) {
                    break;
                }
                if (spins >= (unsigned)__builtin_amdgcn_readfirstlane(cap)) { cap = 0; break; }
            }
            STAMP(5);
            *(u32x4*)(l_mem + wave * 2048 + lane * 16) = y0;
            *(u32x4*)(l_mem + wave * 2048 + 1024 + lane * 16) = y1;
        }
        PH(1);
        asm volatile("s_waitcnt vmcnt(0)" ::: "memory");
        __syncthreads();
        if (part == 0 && wave == 0 && get_lane() == 0) {
            const int u = unit_of_last_seg(s);
            if (u >= 0) __hip_atomic_fetch_add(cnt_ptr(ws, u, 0), 1u, __ATOMIC_RELAXED, __HIP_MEMORY_SCOPE_AGENT);
        }
    }
}

extern "C" void kernel_launch(void* const* d_in, const int* in_sizes, int n_in, void* d_out, int out_size, void* d_ws,
                              size_t ws_size, hipStream_t stream) {
    const unsigned short* x = (const unsigned short*)d_in[0];
    const unsigned short* mem0 = (const unsigned short*)d_in[1];
    const unsigned short* Wq = (const unsigned short*)d_in[2];
    const unsigned short* Wk = (const unsigned short*)d_in[3];
    const unsigned short* Wv = (const unsigned short*)d_in[4];
    const unsigned short* Wo = (const unsigned short*)d_in[5];
    const unsigned short* Wg = (const unsigned short*)d_in[8];
    const unsigned short* Wu = (const unsigned short*)d_in[9];
    const unsigned short* Wd = (const unsigned short*)d_in[10];
    char* ws = (char*)d_ws;
    prep_kernel<<<NB_PREP, 256, 0, stream>>>(x, Wq, Wk, Wv, Wo, Wg, Wu, Wd, ws);
    chain_kernel<<<NCW + NRW + NWK, 256, 0, stream>>>(mem0, x, (unsigned short*)d_out, ws, Wg, Wu, Wd, Wq, Wk, Wv, Wo);
}
```

```cpp
#include <hip/hip_runtime.h>
#include <hip/hip_bf16.h>
#include <stdint.h>
#include <type_traits>

constexpr int S_ = 2048, D_ = 512, H_ = 32, DH_ = 128, M_ = 8, SEG_ = 32, NSEG_ = 64, F_ = 1792;

typedef short bf16x8 __attribute__((ext_vector_type(8)));
typedef short bf16x4 __attribute__((ext_vector_type(4)));
typedef float f32x4 __attribute__((ext_vector_type(4)));
typedef unsigned u32x4 __attribute__((ext_vector_type(4)));
typedef unsigned u32x2 __attribute__((ext_vector_type(2)));
typedef __bf16 bfv2 __attribute__((ext_vector_type(2)));

__device__ __forceinline__ unsigned pack2(float a, float b) {
    bfv2 v = {(__bf16)a, (__bf16)b};
    return __builtin_bit_cast(unsigned, v);
}
__device__ __forceinline__ float bf16r(float a) { return (float)(__bf16)a; }
__device__ __forceinline__ float bflo(unsigned u) { return __uint_as_float(u << 16); }
__device__ __forceinline__ float bfhi(unsigned u) { return __uint_as_float(u & 0xffff0000u); }
__device__ __forceinline__ bf16x8 pack8(f32x4 a, f32x4 b) {
    u32x4 r = {pack2(a[0], a[1]), pack2(a[2], a[3]), pack2(b[0], b[1]), pack2(b[2], b[3])};
    return __builtin_bit_cast(bf16x8, r);
}
__device__ __forceinline__ bf16x4 pack4(f32x4 a) {
    u32x2 r = {pack2(a[0], a[1]), pack2(a[2], a[3])};
    return __builtin_bit_cast(bf16x4, r);
}
__device__ __forceinline__ int get_lane() {
    int l;
    asm volatile("v_mbcnt_lo_u32_b32 %0, -1, 0\n\tv_mbcnt_hi_u32_b32 %0, -1, %0" : "=v"(l));
    return l;
}
#define MFMA32(a, b, c) __builtin_amdgcn_mfma_f32_16x16x32_bf16(a, b, c, 0, 0, 0)
__device__ __forceinline__ bf16x8 ext8(bf16x4 a) { bf16x8 r = {a[0], a[1], a[2], a[3], 0, 0, 0, 0}; return r; }
#define MFMA16(a, b, c) MFMA32(ext8(a), ext8(b), c)

constexpr size_t MB = 1024 * 1024;
constexpr size_t OFF_WQKV = 0;
constexpr size_t OFF_WO = OFF_WQKV + 12 * MB;
constexpr size_t OFF_WGU = OFF_WO + 4 * MB;
constexpr size_t OFF_WD = OFF_WGU + 4 * MB;
constexpr size_t OFF_XN = OFF_WD + 2 * MB;
constexpr size_t OFF_QF = OFF_XN + 2 * MB;
constexpr size_t OFF_KF = OFF_QF + 16 * MB;
constexpr size_t OFF_VF = OFF_KF + 16 * MB;
constexpr size_t OFF_KM = OFF_VF + 16 * MB;
constexpr size_t OFF_VM = OFF_KM + 8 * MB;
constexpr size_t OFF_OF = OFF_VM + 8 * MB;
constexpr size_t OFF_P1 = OFF_OF + 16 * MB;
constexpr size_t OFF_H = OFF_P1 + 32 * MB;
constexpr size_t OFF_HN = OFF_H + 2 * MB;
constexpr size_t OFF_AF = OFF_HN + 2 * MB;
constexpr size_t OFF_P2 = OFF_AF + 8 * MB;
constexpr size_t OFF_PART = OFF_P2 + 16 * MB;
constexpr size_t OFF_MEMV = OFF_PART + 1 * MB;
constexpr size_t OFF_CNT = OFF_MEMV + 4 * MB;
constexpr size_t WS_END = OFF_CNT + 1 * MB;

__device__ __forceinline__ float wave_sum(float v) {
#pragma unroll
    for (int m = 1; m < 64; m <<= 1) v += __shfl_xor(v, m);
    return v;
}
__device__ __forceinline__ u32x4 ln_row(const float (&x)[8]) {
    float s = 0.f;
#pragma unroll
    for (int j = 0; j < 8; ++j) s += x[j];
    const float mean = wave_sum(s) * (1.0f / 512.0f);
    float q = 0.f;
#pragma unroll
    for (int j = 0; j < 8; ++j) { const float d = x[j] - mean; q += d * d; }
    const float var = wave_sum(q) * (1.0f / 512.0f);
    const float mb = bf16r(mean);
    const float vb = bf16r(var);
    const float den = bf16r(sqrtf(bf16r(vb + 1e-5f)));
    float y[8];
#pragma unroll
    for (int j = 0; j < 8; ++j) y[j] = bf16r(x[j] - mb) / den;
    u32x4 r = {pack2(y[0], y[1]), pack2(y[2], y[3]), pack2(y[4], y[5]), pack2(y[6], y[7])};
    return r;
}
__device__ __forceinline__ void store_row_frag(char* base, int row, int lane, u32x4 v) {
    const int tt = row >> 4, c = row & 15, ks = lane >> 2, g = lane & 3;
    *(u32x4*)(base + ((size_t)(tt * 16 + ks) * 64 + g * 16 + c) * 16) = v;
}

constexpr int NB_LN = 512, NB_CLR = 64 + 256 + 8;
constexpr int NB_PREP = NB_LN + NB_CLR;

__global__ void __launch_bounds__(256) prep_kernel(const unsigned short* __restrict__ x, char* __restrict__ ws) {
    int b = blockIdx.x;
    const int tid = threadIdx.x, wave = tid >> 6, lane = tid & 63;
    if (b < NB_LN) {
        const int row = b * 4 + wave;
        const u32x4 v = *(const u32x4*)(x + (size_t)row * 512 + lane * 8);
        float f[8] = {bflo(v[0]), bfhi(v[0]), bflo(v[1]), bfhi(v[1]), bflo(v[2]), bfhi(v[2]), bflo(v[3]), bfhi(v[3])};
        store_row_frag(ws + OFF_XN, row, lane, ln_row(f));
    } else {
        b -= NB_LN;
        const bool p = b < 64 || b >= 320;
        char* d = b < 64 ? ws + OFF_PART + (size_t)b * 16384 : ws + OFF_MEMV + (size_t)(b - 64) * 16384;
        const unsigned f = p ? 0u : 0xFFFFFFFFu;
        const u32x4 v = {f, f, f, f};
#pragma unroll
        for (int i = 0; i < 4; ++i) *(u32x4*)(d + i * 4096 + tid * 16) = v;
    }
}

#define TR_READ(p) __builtin_amdgcn_ds_read_tr16_b64_v4i16((__attribute__((address_space(3))) bf16x4*)(p))
template <int B0, int B1>
__device__ __forceinline__ void frags_issue(const unsigned short* src, bf16x8 (&f)[2][16]) {
    const int lane = get_lane();
    const unsigned short* sp = src + (size_t)(lane >> 2) * 4096 + (lane & 3) * 8;
#pragma unroll
    for (int b = B0; b < B1; ++b)
#pragma unroll
        for (int i = 0; i < 4; ++i) f[i & 1][2 * b + (i >> 1)] = *(const bf16x8*)(sp + (size_t)(b * 64 + i * 16) * 4096);
    __builtin_amdgcn_sched_barrier(0);
}
template <bool PIN_A, int B0, int B1>
__device__ __forceinline__ void frags_transpose(char* scr, bf16x8 (&f)[2][16]) {
    const int lane = get_lane();
    const int c = lane & 15, g = lane >> 4;
#pragma unroll
    for (int b = B0; b < B1; ++b) {
#pragma unroll
        for (int i = 0; i < 4; ++i) *(bf16x8*)(scr + (i * 16 + (lane >> 2)) * 80 + (lane & 3) * 16) = f[i & 1][2 * b + (i >> 1)];
        asm volatile("s_waitcnt lgkmcnt(0)" ::: "memory");
#pragma unroll
        for (int kk = 0; kk < 2; ++kk)
#pragma unroll
            for (int t = 0; t < 2; ++t) {
                const char* ap = scr + (kk * 32 + 8 * g + (c >> 2)) * 80 + (t * 16 + 4 * (c & 3)) * 2;
                const bf16x4 lo4 = TR_READ(ap), hi4 = TR_READ(ap + 4 * 80);
                bf16x8 r = {lo4[0], lo4[1], lo4[2], lo4[3], hi4[0], hi4[1], hi4[2], hi4[3]};
                if constexpr (PIN_A) asm volatile("" : "+a"(r)); else asm volatile("" : "+v"(r));
                f[t][2 * b + kk] = r;
            }
        __builtin_amdgcn_sched_barrier(0);
        asm volatile("s_waitcnt lgkmcnt(0)" ::: "memory");
    }
}

enum { G_KV = 0, G_OUTP = 1, G_FFN1 = 2, G_DOWN = 3, G_Q = 4 };

__device__ __forceinline__ void st16_dev(const __amdgpu_buffer_rsrc_t rs, size_t off, u32x4 v) {
    __builtin_amdgcn_raw_buffer_store_b128(v, rs, (unsigned)off, 0, 16);
}

template <int MODE, int TT, class SYNC>
__device__ __forceinline__ void gemm_core(char* ws, char* xl, const __amdgpu_buffer_rsrc_t wrs, int tt0, int cb, int wave, SYNC sync) {
    int lane;
    constexpr int NKS = MODE == G_DOWN ? 14 : 16;
    constexpr int KST = MODE == G_OUTP ? 128 : (MODE == G_DOWN ? 56 : 16);
    constexpr int NT = MODE == G_FFN1 ? 4 : 2;
    constexpr int NIT = 1;
    lane = get_lane();
    int ks0 = 0, cg = cb;
    const char* xsrc; unsigned wsrc;
    if (MODE == G_KV || MODE == G_Q) { xsrc = ws + OFF_XN; wsrc = (unsigned)OFF_WQKV; }
    else if (MODE == G_OUTP) { xsrc = ws + OFF_OF; wsrc = (unsigned)OFF_WO; ks0 = (cb & 7) * 16; cg = cb >> 3; }
    else if (MODE == G_FFN1) { xsrc = ws + OFF_HN; wsrc = (unsigned)OFF_WGU; }
    else { xsrc = ws + OFF_AF; wsrc = (unsigned)OFF_WD; ks0 = (cb & 3) * 14; cg = cb >> 2; }
    const int c = lane & 15, g = lane >> 4;
    auto tile0 = [&](int it) -> int {
        if (MODE == G_KV) { const int ci = 32 + cg; return (ci >> 5) * 256 + (ci & 31) * 8 + wave * 2; }
        else if (MODE == G_Q) return cg * 8 + wave * 2;
        else if (MODE == G_FFN1) return (cg * 4 + wave) * 4;
        else return cg * 8 + wave * 2;
    };
    constexpr int PD = NKS / 2;
    bf16x8 wr[PD][NT];
    const unsigned wvo = wsrc + (unsigned)((tile0(0) * KST + ks0) * 64 + lane) * 16;
#define LD_W(t, ks) __builtin_bit_cast(bf16x8, __builtin_amdgcn_raw_buffer_load_b128(wrs, wvo, ((t) * KST + (ks)) * 1024, 16))
#pragma unroll
    for (int ks = 0; ks < PD; ++ks)
#pragma unroll
        for (int t = 0; t < NT; ++t) wr[ks][t] = LD_W(t, ks);
    sync();
#pragma unroll
    for (int j = 0; j < (TT * NKS + 3) / 4; ++j) {
        const int i = j * 4 + wave;
        if (i < TT * NKS) {
            const int tt = i / NKS, ks = i % NKS;
            if constexpr (MODE == G_KV || MODE == G_Q)
                __builtin_amdgcn_global_load_lds((const unsigned*)(xsrc + ((size_t)((tt0 + tt) * KST + ks0 + ks) * 64 + lane) * 16),
                                                 (unsigned*)(xl + (size_t)i * 1024 + lane * 16), 16, 0, 0);
            else
                __builtin_amdgcn_global_load_lds((const unsigned*)(xsrc + ((size_t)((tt0 + tt) * KST + ks0 + ks) * 64 + lane) * 16),
                                                 (unsigned*)(xl + (size_t)i * 1024 + lane * 16), 16, 0, 16);
        }
    }
    asm volatile("s_waitcnt vmcnt(0)" ::: "memory");
    __syncthreads();
#pragma unroll 1
    for (int it = 0; it < NIT; ++it) {
        asm volatile("" ::: "memory");
        const int nt0 = tile0(it);
        int mat = 0, head = 0;
        if (MODE == G_KV) { const int ci = 32 + cg; mat = ci >> 5; head = ci & 31; }
        f32x4 acc[NT][TT];
#pragma unroll
        for (int t = 0; t < NT; ++t)
#pragma unroll
            for (int tt = 0; tt < TT; ++tt) acc[t][tt] = (f32x4){0.f, 0.f, 0.f, 0.f};
        const bool vmat = (MODE == G_KV) && (mat == 2);
        auto kloop = [&](auto swapped) {
            bf16x8 xf[2][TT];
#pragma unroll
            for (int tt = 0; tt < TT; ++tt) xf[0][tt] = *(const bf16x8*)(xl + (tt * NKS) * 1024 + lane * 16);
#pragma unroll
            for (int ks = 0; ks < NKS; ++ks) {
                bf16x8 wf[NT];
#pragma unroll
                for (int t = 0; t < NT; ++t) {
                    wf[t] = wr[ks % PD][t];
                    if (ks + PD < NKS) wr[ks % PD][t] = LD_W(t, ks + PD);
                }
                if (ks + 1 < NKS) {
#pragma unroll
                    for (int tt = 0; tt < TT; ++tt) xf[(ks + 1) & 1][tt] = *(const bf16x8*)(xl + (tt * NKS + ks + 1) * 1024 + lane * 16);
                }
                __builtin_amdgcn_sched_barrier(0);
#pragma unroll
                for (int tt = 0; tt < TT; ++tt)
#pragma unroll
                    for (int t = 0; t < NT; ++t) {
                        if constexpr (decltype(swapped)::value) acc[t][tt] = MFMA32(xf[ks & 1][tt], wf[t], acc[t][tt]);
                        else acc[t][tt] = MFMA32(wf[t], xf[ks & 1][tt], acc[t][tt]);
                    }
                __builtin_amdgcn_sched_barrier(0);
            }
        };
        if (vmat) kloop(std::true_type{}); else kloop(std::false_type{});
        if (MODE == G_KV) {
            if (mat == 2) {
#pragma unroll
                for (int sg = 0; sg < TT / 2; ++sg)
#pragma unroll
                    for (int t = 0; t < 2; ++t) {
                        const int seg = tt0 / 2 + sg;
                        st16_dev(wrs, OFF_VF + ((size_t)(((seg * 32 + head) * 4 + wave) * 2 + t) * 64 + lane) * 16, __builtin_bit_cast(u32x4, pack8(acc[t][2 * sg], acc[t][2 * sg + 1])));
                    }
            } else {
#pragma unroll
                for (int tt = 0; tt < TT; ++tt) {
                    const int seg = tt0 / 2 + (tt >> 1);
                    st16_dev(wrs, OFF_KF + ((size_t)(((seg * 32 + head) * 4 + wave) * 2 + (tt & 1)) * 64 + lane) * 16, __builtin_bit_cast(u32x4, pack8(acc[0][tt], acc[1][tt])));
                }
            }
        } else if (MODE == G_Q) {
            __syncthreads();
#pragma unroll
            for (int tt = 0; tt < TT; ++tt) *(bf16x8*)(xl + (tt * 4 + wave) * 1024 + lane * 16) = pack8(acc[0][tt], acc[1][tt]);
        } else if (MODE == G_OUTP || MODE == G_DOWN) {
            const size_t dst = (MODE == G_OUTP ? OFF_P1 : OFF_P2) + (size_t)(MODE == G_OUTP ? (cb & 7) : (cb & 3)) * S_ * D_ * 4;
#pragma unroll
            for (int t = 0; t < 2; ++t)
#pragma unroll
                for (int tt = 0; tt < TT; ++tt)
                    st16_dev(wrs, dst + ((size_t)((tt0 + tt) * 16 + c) * D_ + (nt0 + t) * 16 + g * 4) * 4, __builtin_bit_cast(u32x4, acc[t][tt]));
        } else {
            const int ag = cg * 4 + wave;
#pragma unroll
            for (int tt = 0; tt < TT; ++tt) {
                f32x4 a0, a1;
#pragma unroll
                for (int i = 0; i < 4; ++i) {
                    a0[i] = bf16r(acc[0][tt][i]) * bf16r(acc[2][tt][i]);
                    a1[i] = bf16r(acc[1][tt][i]) * bf16r(acc[NT - 1][tt][i]);
                }
                st16_dev(wrs, OFF_AF + ((size_t)((tt0 + tt) * 56 + ag) * 64 + lane) * 16, __builtin_bit_cast(u32x4, pack8(a0, a1)));
            }
        }
    }
}

constexpr float SCALE_ = 0.08838834764831845f;
constexpr float LOG2E_ = 1.4426950408889634f;
constexpr unsigned SPIN_CAP = 1u << 17;

__device__ __forceinline__ float max_x16(float v) {
    auto r = __builtin_amdgcn_permlane16_swap(__float_as_uint(v), __float_as_uint(v), false, false);
    return fmaxf(__uint_as_float(r[0]), __uint_as_float(r[1]));
}
__device__ __forceinline__ float max_x32(float v) {
    auto r = __builtin_amdgcn_permlane32_swap(__float_as_uint(v), __float_as_uint(v), false, false);
    return fmaxf(__uint_as_float(r[0]), __uint_as_float(r[1]));
}
__device__ __forceinline__ float add_x16(float v) {
    auto r = __builtin_amdgcn_permlane16_swap(__float_as_uint(v), __float_as_uint(v), false, false);
    return __uint_as_float(r[0]) + __uint_as_float(r[1]);
}
__device__ __forceinline__ float add_x32(float v) {
    auto r = __builtin_amdgcn_permlane32_swap(__float_as_uint(v), __float_as_uint(v), false, false);
    return __uint_as_float(r[0]) + __uint_as_float(r[1]);
}
__device__ __forceinline__ float add_x8(float v) {
    return v + __uint_as_float(__builtin_amdgcn_update_dpp(0u, __float_as_uint(v), 0x128, 0xf, 0xf, false));
}
#define MFMA_AA(acc, wa, b) asm volatile("v_mfma_f32_16x16x32_bf16 %0, %1, %2, %0" : "+v"(acc) : "a"(wa), "v"(b))
#define MFMA_AA0(acc, wa, b) asm volatile("v_mfma_f32_16x16x32_bf16 %0, %1, %2, 0" : "=&v"(acc) : "a"(wa), "v"(b))
#define MFMA_VV(acc, a, b) asm volatile("v_mfma_f32_16x16x32_bf16 %0, %1, %2, %0" : "+v"(acc) : "v"(a), "v"(b))
#define MFMA_VV0(acc, a, b) asm volatile("v_mfma_f32_16x16x32_bf16 %0, %1, %2, 0" : "=&v"(acc) : "v"(a), "v"(b))
#define NOP_IN2(a, b) asm volatile("s_nop 1" : "+v"(a), "+v"(b))
#define NOP_OUT2(a, b) asm volatile("s_nop 7\n\ts_nop 3" : "+v"(a), "+v"(b))
#define NOP_OUT4(a, b, c, d) asm volatile("s_nop 7\n\ts_nop 3" : "+v"(a), "+v"(b), "+v"(c), "+v"(d))

#define CPH 2
constexpr int NCW = 32 * CPH;
constexpr int TPW = 8 / CPH;
__device__ __forceinline__ float max_x8(float v) {
    return fmaxf(v, __uint_as_float(__builtin_amdgcn_update_dpp(0u, __float_as_uint(v), 0x128, 0xf, 0xf, false)));
}
__device__ __forceinline__ unsigned ror8(unsigned v) { return __builtin_amdgcn_update_dpp(0u, v, 0x128, 0xf, 0xf, false); }

#define NCOPY 8
#define SLBIG 32
#define SLCH 127
#define GP 352
#define GE 25
#define GG 105
#define RLO 20
#define CHI 60
#define GK 2
#define SLMID 20
#define GPM 336
#define GT1 (20 * GP)
#define GT2 (GT1 + 40 * GPM)
#define RWV 2
constexpr int NRW = 128 / RWV;
constexpr int NWK = 128;
constexpr int NUNIT = 21;
__device__ __forceinline__ int unit_of_last_seg(int s) {
    if (s < 52) return (s & 3) == 3 ? (s >> 2) : -1;
    if (s < 60) return (s & 1) ? 13 + ((s - 52) >> 1) : -1;
    return 17 + (s - 60);
}
constexpr unsigned PROG = (unsigned)(OFF_CNT + 32768);
__device__ __forceinline__ void wait_word(const __amdgpu_buffer_rsrc_t rs, unsigned off, unsigned want, unsigned cap) {
    cap = (unsigned)__builtin_amdgcn_readfirstlane(cap);
    for (unsigned spins = 0; spins <= cap; ++spins) {
        asm volatile("" : "+v"(off)::"memory");
        const unsigned v = __builtin_amdgcn_raw_buffer_load_b32(rs, off, 0, 16);
        if ((unsigned)__builtin_amdgcn_readfirstlane(v) >= want) break;
    }
}
__device__ __forceinline__ void wait_grid(unsigned t0, unsigned off, int lo, int hi, bool addp) {
    const unsigned t = (unsigned)__builtin_amdgcn_s_memrealtime() - t0;
    unsigned d, l;
    if (t >= GT1 && t < GT2) {
        constexpr unsigned DL = GPM / GK;
        l = lo + (addp ? GPM : 0);
        d = l + DL - 1 - (t - GT1 + l + 8 * GPM + DL - 1 - off) % DL;
        l = hi + (addp ? GPM : 0);
    } else {
        constexpr unsigned DL = GP / GK;
        l = lo + (addp ? GP : 0);
        d = l + DL - 1 - (t - (t >= GT2 ? GT2 : 0) + l + 8 * GP + DL - 1 - off) % DL;
        l = hi + (addp ? GP : 0);
    }
    if (d <= l) {
        const unsigned target = t + d;
        while ((unsigned)__builtin_amdgcn_s_memrealtime() - t0 < target) { }
    }
}
typedef __attribute__((address_space(1))) unsigned gu32;
__device__ __forceinline__ gu32* cnt_ptr(char* ws, int u, int ph) { return (gu32*)(ws + OFF_CNT + (size_t)(u * 8 + ph) * 128); }
__device__ __forceinline__ void arrive(gu32* c, int wave) {
    asm volatile("s_waitcnt vmcnt(0)" ::: "memory");
    __syncthreads();
    if (wave == 0 && get_lane() == 0) __hip_atomic_fetch_add(c, 1u, __ATOMIC_RELAXED, __HIP_MEMORY_SCOPE_AGENT);
}
template <int SLP>
__device__ __forceinline__ void wait_for(gu32* c, unsigned target, int wave, unsigned& cap) {
    if (wave == 0) {
        for (unsigned spins = 0;; ++spins) {
            if ((unsigned)__builtin_amdgcn_readfirstlane(__hip_atomic_load(c, __ATOMIC_RELAXED, __HIP_MEMORY_SCOPE_AGENT)) >= target) break;
            if (spins >= cap) { cap = 0; break; }
            __builtin_amdgcn_s_sleep(SLP);
        }
    }
    asm volatile("" ::: "memory");
    __syncthreads();
}

__device__ __forceinline__ unsigned flag_off(int u, int ph, int wg) { return (unsigned)(OFF_CNT + 65536 + (size_t)(((u & 1) * 8 + ph) * 32 + wg) * 128); }
__device__ __forceinline__ void arrive_f(const __amdgpu_buffer_rsrc_t wrs, int u, int ph, int wg, int wave) {
    asm volatile("s_waitcnt vmcnt(0)" ::: "memory");
    __syncthreads();
    if (wave == 0) __builtin_amdgcn_raw_buffer_store_b32(1u, wrs, get_lane() == 0 ? flag_off(u, ph, wg) : 0x40000000u, 0, 16);
}
__device__ __forceinline__ void wait_f(const __amdgpu_buffer_rsrc_t wrs, int u, int ph, int wave, unsigned& cap) {
    if (wave == 0) {
        unsigned off = flag_off(u, ph, get_lane() & 31);
        for (unsigned spins = 0;; ++spins) {
            asm volatile("" : "+v"(off)::"memory");
            const unsigned v = __builtin_amdgcn_raw_buffer_load_b32(wrs, off, 0, 16);
            if (__all(v != 0u)) break;
            if (spins >= (unsigned)__builtin_amdgcn_readfirstlane(cap)) { cap = 0; break; }
        }
    }
    asm volatile("" ::: "memory");
    __syncthreads();
}

#define LD16_DEV(off) __builtin_bit_cast(bf16x8, __builtin_amdgcn_raw_buffer_load_b128(wrs, (unsigned)(off), 0, 16))
#define LD8_DEV(off) __builtin_bit_cast(bf16x4, __builtin_amdgcn_raw_buffer_load_b64(wrs, (unsigned)(off), 0, 16))
template <int Q0, int Q1>
__device__ __forceinline__ void seg_attention(const char* qf, const __amdgpu_buffer_rsrc_t wrs, int seg, int h,
                                              const bf16x8 (&kf)[8], const bf16x8 (&vf)[8]) {
    const int lane = get_lane();
    const int c = lane & 15, g = lane >> 4;
    bf16x8 km[4]; bf16x4 vm[8];
#pragma unroll
    for (int w = 0; w < 4; ++w) km[w] = LD16_DEV(OFF_KM + ((size_t)((seg * 32 + h) * 4 + w) * 64 + lane) * 16);
#pragma unroll
    for (int i = 0; i < 8; ++i) vm[i] = LD8_DEV(OFF_VM + ((size_t)((seg * 32 + h) * 8 + i) * 64 + lane) * 8);
    __builtin_amdgcn_sched_barrier(0);
    const f32x4 z = {0, 0, 0, 0};
    f32x4 sc[2][3];
#pragma unroll
    for (int qt = 0; qt < 2; ++qt)
#pragma unroll
        for (int kt = 0; kt < 3; ++kt) sc[qt][kt] = z;
    bf16x8 q[2][4];
#pragma unroll
    for (int w = 0; w < 4; ++w) {
#pragma unroll
        for (int qt = Q0; qt < Q1; ++qt) {
            q[qt][w] = *(const bf16x8*)(qf + (qt * 4 + w) * 1024 + lane * 16);
            sc[qt][0] = MFMA32(kf[w * 2], q[qt][w], sc[qt][0]); sc[qt][1] = MFMA32(kf[w * 2 + 1], q[qt][w], sc[qt][1]);
        }
    }
#pragma unroll
    for (int w = 0; w < 4; ++w)
#pragma unroll
        for (int qt = Q0; qt < Q1; ++qt) sc[qt][2] = MFMA32(km[w], q[qt][w], sc[qt][2]);
    bf16x8 pB0[2]; bf16x4 pB1[2];
#pragma unroll
    for (int qt = Q0; qt < Q1; ++qt) {
        const int qi = qt * 16 + c;
        float mx = -3.0e38f;
#pragma unroll
        for (int kt = 0; kt < 3; ++kt)
#pragma unroll
            for (int i = 0; i < 4; ++i) {
                float v = bf16r(bf16r(sc[qt][kt][i]) * SCALE_);
                const bool valid = kt == 2 ? (g < 2) : (kt * 16 + 4 * g + i <= qi);
                v = valid ? v : -3.0e38f;
                sc[qt][kt][i] = v;
                mx = fmaxf(mx, v);
            }
        mx = max_x32(max_x16(mx));
        float sum = 0.f;
#pragma unroll
        for (int kt = 0; kt < 3; ++kt)
#pragma unroll
            for (int i = 0; i < 4; ++i) {
                const bool valid = kt == 2 ? (g < 2) : (kt * 16 + 4 * g + i <= qi);
                const float e = valid ? __builtin_amdgcn_exp2f((sc[qt][kt][i] - mx) * LOG2E_) : 0.f;
                sc[qt][kt][i] = e;
                sum += e;
            }
        sum = add_x32(add_x16(sum));
        const float inv = __builtin_amdgcn_rcpf(sum);
#pragma unroll
        for (int kt = 0; kt < 3; ++kt) sc[qt][kt] *= inv;
        pB0[qt] = pack8(sc[qt][0], sc[qt][1]);
        pB1[qt] = pack4(sc[qt][2]);
    }
#pragma unroll
    for (int w = 0; w < 4; ++w) {
#pragma unroll
        for (int qt = Q0; qt < Q1; ++qt) {
            f32x4 o0 = MFMA32(vf[w * 2], pB0[qt], z);
            f32x4 o1 = MFMA32(vf[w * 2 + 1], pB0[qt], z);
            o0 = MFMA16(vm[w * 2], pB1[qt], o0);
            o1 = MFMA16(vm[w * 2 + 1], pB1[qt], o1);
            st16_dev(wrs, OFF_OF + ((size_t)((seg * 2 + qt) * 128 + h * 4 + w) * 64 + lane) * 16, __builtin_bit_cast(u32x4, pack8(o0, o1)));
        }
    }
}

__device__ __forceinline__ void kv_phase(const unsigned short* Wk, const unsigned short* Wv, char* ws, char* xl, const __amdgpu_buffer_rsrc_t wrs,
                                         int wk, int wave) {
    const int ci = wk & 63, vmat = ci >> 5, head = ci & 31;
    bf16x8 wr[2][16];
    frags_issue<0, 8>((vmat ? Wv : Wk) + head * 128 + wave * 32, wr);
    frags_transpose<false, 0, 8>(xl + wave * 8192, wr);
#pragma unroll 1
    for (int i = 0; i < 8; ++i) {
        const int rb = 2 * i + (wk >> 6);
        const int lane = get_lane();
        __syncthreads();
#pragma unroll
        for (int j = 0; j < 32; ++j) {
            const int q = j * 4 + wave;
            __builtin_amdgcn_global_load_lds((const unsigned*)(ws + OFF_XN + ((size_t)(rb * 128 + q) * 64 + lane) * 16),
                                             (unsigned*)(xl + (size_t)q * 1024 + lane * 16), 16, 0, 0);
        }
        asm volatile("s_waitcnt vmcnt(0)" ::: "memory");
        __syncthreads();
        f32x4 acc[2][8];
#pragma unroll
        for (int t = 0; t < 2; ++t)
#pragma unroll
            for (int tt = 0; tt < 8; ++tt) acc[t][tt] = (f32x4){0.f, 0.f, 0.f, 0.f};
        auto kloop = [&](auto swapped) {
            bf16x8 xf[2][8];
#pragma unroll
            for (int tt = 0; tt < 8; ++tt) xf[0][tt] = *(const bf16x8*)(xl + (tt * 16) * 1024 + lane * 16);
#pragma unroll
            for (int ks = 0; ks < 16; ++ks) {
                if (ks + 1 < 16) {
#pragma unroll
                    for (int tt = 0; tt < 8; ++tt) xf[(ks + 1) & 1][tt] = *(const bf16x8*)(xl + (tt * 16 + ks + 1) * 1024 + lane * 16);
                }
                __builtin_amdgcn_sched_barrier(0);
#pragma unroll
                for (int tt = 0; tt < 8; ++tt)
#pragma unroll
                    for (int t = 0; t < 2; ++t) {
                        if constexpr (decltype(swapped)::value) acc[t][tt] = MFMA32(xf[ks & 1][tt], wr[t][ks], acc[t][tt]);
                        else acc[t][tt] = MFMA32(wr[t][ks], xf[ks & 1][tt], acc[t][tt]);
                    }
                __builtin_amdgcn_sched_barrier(0);
            }
        };
        if (vmat) {
            kloop(std::true_type{});
#pragma unroll
            for (int sg = 0; sg < 4; ++sg)
#pragma unroll
                for (int t = 0; t < 2; ++t)
                    st16_dev(wrs, OFF_VF + ((size_t)((((rb * 4 + sg) * 32 + head) * 4 + wave) * 2 + t) * 64 + lane) * 16, __builtin_bit_cast(u32x4, pack8(acc[t][2 * sg], acc[t][2 * sg + 1])));
        } else {
            kloop(std::false_type{});
#pragma unroll
            for (int tt = 0; tt < 8; ++tt)
                st16_dev(wrs, OFF_KF + ((size_t)((((rb * 4 + (tt >> 1)) * 32 + head) * 4 + wave) * 2 + (tt & 1)) * 64 + lane) * 16, __builtin_bit_cast(u32x4, pack8(acc[0][tt], acc[1][tt])));
        }
        arrive(cnt_ptr(ws, rb, 6), wave);
    }
}

template <int TT, bool FAST>
__device__ __forceinline__ void worker_unit(const unsigned short* x, unsigned short* out, char* ws, char* lds, const __amdgpu_buffer_rsrc_t wrs,
                                            int u, int seg0, int wg, int wave, unsigned& cap) {
    constexpr int SL = FAST ? 2 : (TT > 4 ? SLBIG : SLMID);
    const int tt0 = seg0 * 2;
#define ARR(ph) do { if constexpr (FAST) arrive_f(wrs, u, ph, wg, wave); else arrive(cnt_ptr(ws, u, ph), wave); } while (0)
#define WAI(ph) do { if constexpr (FAST) wait_f(wrs, u, ph, wave, cap); else wait_for<SL>(cnt_ptr(ws, u, ph), 32, wave, cap); } while (0)
    wait_for<16>(cnt_ptr(ws, 30, 0), NRW * (4 - RWV), wave, cap);
    gemm_core<G_Q, TT>(ws, lds, wrs, tt0, wg, wave, [] {});
    {
        wait_for<16>(cnt_ptr(ws, 30, 2), NRW * (4 - RWV), wave, cap);
        wait_for<16>(cnt_ptr(ws, seg0 >> 2, 6), 64, wave, cap);
        bf16x8 kf[8], vf[8];
        const int sw = wave < TT / 2 ? wave : 0;
        {
            const int lane = get_lane();
            const unsigned fb = (unsigned)(OFF_KF + ((size_t)((seg0 + sw) * 32 + wg) * 8 * 64 + lane) * 16);
#pragma unroll
            for (int i = 0; i < 8; ++i) { kf[i] = LD16_DEV(fb + i * 1024); vf[i] = LD16_DEV(fb + 16 * MB + i * 1024); }
        }
        if constexpr (FAST) {
            if (wave < 2) {
                wait_word(wrs, PROG + wg * 128, seg0 + 1, cap);
                if (wave == 0) seg_attention<0, 1>(lds, wrs, seg0, wg, kf, vf); else seg_attention<1, 2>(lds, wrs, seg0, wg, kf, vf);
            }
        } else {
            wait_for<(TT > 4 ? SLCH : 32)>((gu32*)(ws + PROG + wg * 128), seg0 + TT / 2, wave, cap);
            if (wave < TT / 2) seg_attention<0, 2>(lds + wave * 8192, wrs, seg0 + wave, wg, kf, vf);
        }
    }
    ARR(1);
    gemm_core<G_OUTP, TT>(ws, lds, wrs, tt0, wg, wave, [&] { WAI(1); });
    ARR(2);
    WAI(2);
    const int lrow = wg * 4 + wave;
    const int row = tt0 * 16 + lrow;
    if (lrow < TT * 16) {
        const int lane = get_lane();
        const unsigned po = (unsigned)(OFF_P1 + ((size_t)row * 512 + lane * 8) * 4);
        f32x4 pv[16];
#pragma unroll
        for (int k = 0; k < 8; ++k) {
            pv[2 * k] = __builtin_bit_cast(f32x4, __builtin_amdgcn_raw_buffer_load_b128(wrs, po, k * S_ * D_ * 4, 16));
            pv[2 * k + 1] = __builtin_bit_cast(f32x4, __builtin_amdgcn_raw_buffer_load_b128(wrs, po + 16, k * S_ * D_ * 4, 16));
        }
        const u32x4 v = *(const u32x4*)(x + (size_t)row * 512 + lane * 8);
        __builtin_amdgcn_sched_barrier(0);
        f32x4 a0 = pv[0], a1 = pv[1];
#pragma unroll
        for (int k = 1; k < 8; ++k) { a0 += pv[2 * k]; a1 += pv[2 * k + 1]; }
        float f[8] = {bflo(v[0]), bfhi(v[0]), bflo(v[1]), bfhi(v[1]), bflo(v[2]), bfhi(v[2]), bflo(v[3]), bfhi(v[3])};
#pragma unroll
        for (int j = 0; j < 4; ++j) { f[j] = bf16r(f[j] + bf16r(a0[j])); f[4 + j] = bf16r(f[4 + j] + bf16r(a1[j])); }
        const u32x4 hv = {pack2(f[0], f[1]), pack2(f[2], f[3]), pack2(f[4], f[5]), pack2(f[6], f[7])};
        st16_dev(wrs, OFF_H + ((size_t)row * 512 + lane * 8) * 2, hv);
        st16_dev(wrs, OFF_HN + ((size_t)((row >> 4) * 16 + (lane >> 2)) * 64 + (lane & 3) * 16 + (row & 15)) * 16, ln_row(f));
    }
    ARR(3);
    if (wg < 28) gemm_core<G_FFN1, TT / 2>(ws, lds, wrs, tt0 + (wg / 14) * (TT / 2), wg % 14, wave, [&] { WAI(3); });
    ARR(4);
    gemm_core<G_DOWN, TT / 2>(ws, lds, wrs, tt0 + (wg >> 4) * (TT / 2), wg & 15, wave, [&] { WAI(4); });
    ARR(5);
    WAI(5);
    if (lrow < TT * 16) {
        const int lane = get_lane();
        const size_t i = (size_t)row * 512 + lane * 8;
        const unsigned po = (unsigned)(OFF_P2 + i * 4);
        f32x4 pv[8];
#pragma unroll
        for (int k = 0; k < 4; ++k) {
            pv[2 * k] = __builtin_bit_cast(f32x4, __builtin_amdgcn_raw_buffer_load_b128(wrs, po, k * S_ * D_ * 4, 16));
            pv[2 * k + 1] = __builtin_bit_cast(f32x4, __builtin_amdgcn_raw_buffer_load_b128(wrs, po + 16, k * S_ * D_ * 4, 16));
        }
        const u32x4 v = __builtin_amdgcn_raw_buffer_load_b128(wrs, (unsigned)(OFF_H + i * 2), 0, 16);
        __builtin_amdgcn_sched_barrier(0);
        const f32x4 a0 = (pv[0] + pv[2]) + (pv[4] + pv[6]), a1 = (pv[1] + pv[3]) + (pv[5] + pv[7]);
        const u32x4 o = {pack2(bflo(v[0]) + bf16r(a0[0]), bfhi(v[0]) + bf16r(a0[1])), pack2(bflo(v[1]) + bf16r(a0[2]), bfhi(v[1]) + bf16r(a0[3])),
                         pack2(bflo(v[2]) + bf16r(a1[0]), bfhi(v[2]) + bf16r(a1[1])), pack2(bflo(v[3]) + bf16r(a1[2]), bfhi(v[3]) + bf16r(a1[3]))};
        *(u32x4*)(out + i) = o;
    }
}

__global__ void __launch_bounds__(256, 1) chain_kernel(const unsigned short* mem0, const unsigned short* x, unsigned short* out, char* ws,
                                                      const unsigned short* Wg, const unsigned short* Wu, const unsigned short* Wd,
                                                      const unsigned short* Wq, const unsigned short* Wk, const unsigned short* Wv, const unsigned short* Wo) {
    __shared__ __attribute__((aligned(16))) char lds[CPH > 1 ? 128 * 1024 : 128 * 1024 + 24576];
    char* const l_mem = lds;
    char* const l_sx = l_mem + 8192;
    char* const l_ox = l_sx + 12288;
    char* const l_wo = l_ox + 4096;
    const int wave = __builtin_amdgcn_readfirstlane(threadIdx.x >> 6);
    const int lane = get_lane();
    const int tid = wave * 64 + lane;
    const __amdgpu_buffer_rsrc_t prs = __builtin_amdgcn_make_buffer_rsrc(ws + OFF_PART, 0, 1024 * 1024, 0x00020000);
    const __amdgpu_buffer_rsrc_t mrs = __builtin_amdgcn_make_buffer_rsrc(ws + OFF_MEMV, 0, 64 * 65536, 0x00020000);

    const __amdgpu_buffer_rsrc_t wrs = __builtin_amdgcn_make_buffer_rsrc(ws, 0, (unsigned)WS_END, 0x00020000);
    if (blockIdx.x >= NCW + NRW) {
        const int wk = blockIdx.x - (NCW + NRW), grp = wk >> 5, wg = wk & 31;
        unsigned cap = SPIN_CAP;
        kv_phase(Wk, Wv, ws, lds, wrs, wk, wave);
#pragma unroll 1
        for (int u = grp; u < 13; u += 4) worker_unit<8, false>(x, out, ws, lds, wrs, u, 4 * u, wg, wave, cap);
        {
            const int u = 13 + ((grp + 3) & 3);
            worker_unit<4, false>(x, out, ws, lds, wrs, u, 52 + 2 * (u - 13), wg, wave, cap);
        }
        if (grp == 1 || grp == 2) worker_unit<2, false>(x, out, ws, lds, wrs, 16 + grp, 59 + grp, wg, wave, cap);
        return;
    }
    unsigned gt0;
    {
        const unsigned toff = (unsigned)(OFF_CNT + 31 * 8 * 128);
        if (blockIdx.x == 0 && wave == 0)
            __builtin_amdgcn_raw_buffer_store_b32(((unsigned)__builtin_amdgcn_s_memrealtime() - 1000u) | 1u, wrs, lane == 0 ? toff : 0x40000000u, 0, 16);
        unsigned o = toff; gt0 = 0;
        for (unsigned spins = 0; spins <= SPIN_CAP && gt0 == 0; ++spins) {
            asm volatile("" : "+v"(o)::"memory");
            gt0 = (unsigned)__builtin_amdgcn_readfirstlane(__builtin_amdgcn_raw_buffer_load_b32(wrs, o, 0, 16));
        }
    }
    if (blockIdx.x >= NCW) {
        const int rw = blockIdx.x - NCW;
        unsigned cap = SPIN_CAP;
        if (wave < RWV) {
        const int r = rw * RWV + wave;
        const int t = r >> 2, gh = (r >> 1) & 1, rh = r & 1;
        const unsigned moff = (lane >> 3) < NCOPY ? (unsigned)((lane >> 3) * 8192 + (t >> 1) * 512 + (((t & 1) * 2 + gh) * 8 + 4 * rh) * 16 + (lane & 7) * 8) : 0x40000000u;
        for (int s = 0; s < NSEG_ - 1; ++s) {
            const unsigned tag = ((s >> 1) + 1) & 1;
            unsigned base = ((s & 1) * 128 + r) * 4096 + lane * 16;
            u32x4 x[4];
            if (s > 0) wait_grid(gt0, GE, GE - GG - RLO, 0, true);
            for (unsigned spins = 0;; ++spins) {
                unsigned bad = 0;
                asm volatile("" : "+v"(base)::"memory");
#pragma unroll
                for (int k = 0; k < 4; ++k) x[k] = __builtin_amdgcn_raw_buffer_load_b128(prs, base + k * 1024, 0, 16);
                if (tag) {
                    const u32x4 a4 = (x[0] & x[1]) & (x[2] & x[3]);
                    bad = ~((a4[0] & a4[1]) & (a4[2] & a4[3]));
                } else {
                    const u32x4 o4 = (x[0] | x[1]) | (x[2] | x[3]);
                    bad = (o4[0] | o4[1]) | (o4[2] | o4[3]);
                }
                if (__all((bad & 1u) == 0u)) break;
                if (spins >= (unsigned)__builtin_amdgcn_readfirstlane(cap)) { cap = 0; break; }
            }
            f32x4 a = (__builtin_bit_cast(f32x4, x[0]) + __builtin_bit_cast(f32x4, x[1])) + (__builtin_bit_cast(f32x4, x[2]) + __builtin_bit_cast(f32x4, x[3]));
#pragma unroll
            for (int i = 0; i < 4; ++i) a[i] = add_x32(add_x16(add_x8(a[i])));
            u32x2 o = {pack2(a[0], a[1]), pack2(a[2], a[3])};
            if (o[0] == 0xFFFFFFFFu) o[0] = 0x7FC07FC0u;
            if (o[1] == 0xFFFFFFFFu) o[1] = 0x7FC07FC0u;
            __builtin_amdgcn_raw_buffer_store_b64(o, mrs, (s + 1) * 65536 + moff, 0, 16);
#pragma unroll
            for (int k = 0; k < 4; ++k) __builtin_amdgcn_raw_buffer_store_b128(x[k], prs, base + k * 1024, 0, 16);
        }
        } else {
            unsigned short (*t)[72] = (unsigned short (*)[72])(lds + wave * 8192);
            const int c = lane & 15, g = lane >> 4;
#pragma unroll 1
            for (int ph = 0; ph < 3; ++ph) {
                const int nblk = ph == 2 ? 336 : 256;
#pragma unroll 1
                for (int b = rw * (4 - RWV) + (wave - RWV); b < nblk; b += NRW * (4 - RWV)) {
                    const unsigned short* src; int N, KS, ks, nb, mode; unsigned dst; bool perm = false;
                    if (ph == 0) { src = Wq; N = 4096; KS = 16; ks = b & 15; nb = b >> 4; dst = (unsigned)OFF_WQKV; mode = 0; }
                    else if (ph == 1) { src = Wo; N = 512; KS = 128; ks = b & 127; nb = b >> 7; dst = (unsigned)OFF_WO; mode = 0; perm = true; }
                    else if (b < 224) { const int up = b >= 112, bb = up ? b - 112 : b; src = up ? Wu : Wg; N = 1792; KS = 16; ks = bb & 15; nb = bb >> 4; dst = (unsigned)OFF_WGU; mode = 1 + up; }
                    else { const int bb = b - 224; src = Wd; N = 512; KS = 56; ks = bb % 56; nb = bb / 56; dst = (unsigned)OFF_WD; mode = 0; perm = true; }
                    u32x4 v[4][4];
#pragma unroll
                    for (int sb = 0; sb < 4; ++sb)
#pragma unroll
                        for (int i = 0; i < 4; ++i) v[sb][i] = *(const u32x4*)(src + (size_t)(ks * 32 + i * 8 + (lane >> 3)) * N + nb * 256 + sb * 64 + (lane & 7) * 8);
                    __builtin_amdgcn_sched_barrier(0);
#pragma unroll
                    for (int sb = 0; sb < 4; ++sb) {
#pragma unroll
                        for (int i = 0; i < 4; ++i) *(u32x4*)&t[i * 8 + (lane >> 3)][(lane & 7) * 8] = v[sb][i];
                        asm volatile("s_waitcnt lgkmcnt(0)" ::: "memory");
#pragma unroll
                        for (int q = 0; q < 4; ++q) {
                            const int r0 = (perm ? 4 * g : 8 * g) + (c >> 2), r1 = r0 + (perm ? 16 : 4);
                            const bf16x4 lo4 = TR_READ(&t[r0][q * 16 + 4 * (c & 3)]), hi4 = TR_READ(&t[r1][q * 16 + 4 * (c & 3)]);
                            const bf16x8 o = {lo4[0], lo4[1], lo4[2], lo4[3], hi4[0], hi4[1], hi4[2], hi4[3]};
                            int nt = nb * 16 + sb * 4 + q;
                            if (mode) nt = (nt >> 1) * 4 + (mode == 2 ? 2 : 0) + (nt & 1);
                            st16_dev(wrs, dst + (unsigned)((nt * KS + ks) * 64 + lane) * 16, __builtin_bit_cast(u32x4, o));
                        }
                        asm volatile("s_waitcnt lgkmcnt(0)" ::: "memory");
                    }
                }
                asm volatile("s_waitcnt vmcnt(0)" ::: "memory");
                if (lane == 0) __hip_atomic_fetch_add(cnt_ptr(ws, 30, ph), 1u, __ATOMIC_RELAXED, __HIP_MEMORY_SCOPE_AGENT);
            }
        }
        __syncthreads();
        if (rw < 64) worker_unit<2, true>(x, out, ws, lds, wrs, 19 + (rw >> 5), 62 + (rw >> 5), rw & 31, wave, cap);
        return;
    }
    const int h = blockIdx.x & 31, part = blockIdx.x >> 5;
    const int c = lane & 15, g = lane >> 4;
    bf16x8 wq[2][16], wk[2][16], wv[2][16];
    {
        char* scr = lds + 96 * 1024 + wave * 8192;
        unsigned short (*t)[72] = (unsigned short (*)[72])scr;
        u32x4 v[8 / CPH][4];
#pragma unroll
        for (int nb = 0; nb < 8 / CPH; ++nb)
#pragma unroll
            for (int i = 0; i < 4; ++i) v[nb][i] = *(const u32x4*)(Wo + (size_t)(h * 128 + wave * 32 + i * 8 + (lane >> 3)) * 512 + part * (512 / CPH) + nb * 64 + (lane & 7) * 8);
        frags_issue<0, 8>(Wq + h * 128 + wave * 32, wq);
#pragma unroll
        for (int nb = 0; nb < 8 / CPH; ++nb) {
#pragma unroll
            for (int i = 0; i < 4; ++i) *(u32x4*)&t[i * 8 + (lane >> 3)][(lane & 7) * 8] = v[nb][i];
            asm volatile("s_waitcnt lgkmcnt(0)" ::: "memory");
#pragma unroll
            for (int q = 0; q < 4; ++q) {
                const bf16x4 lo4 = TR_READ(&t[4 * g + (c >> 2)][q * 16 + 4 * (c & 3)]), hi4 = TR_READ(&t[16 + 4 * g + (c >> 2)][q * 16 + 4 * (c & 3)]);
                const bf16x8 o = {lo4[0], lo4[1], lo4[2], lo4[3], hi4[0], hi4[1], hi4[2], hi4[3]};
                *(bf16x8*)(l_wo + ((nb * 4 + q) * 4 + wave) * 1024 + lane * 16) = o;
            }
            asm volatile("s_waitcnt lgkmcnt(0)" ::: "memory");
        }
        __builtin_amdgcn_sched_barrier(0);
        frags_issue<0, 4>(Wk + h * 128 + wave * 32, wk);
        frags_transpose<true, 0, 8>(scr, wq);
        frags_issue<4, 8>(Wk + h * 128 + wave * 32, wk);
        frags_issue<0, 4>(Wv + h * 128 + wave * 32, wv);
        frags_transpose<true, 0, 4>(scr, wk);
        frags_issue<4, 8>(Wv + h * 128 + wave * 32, wv);
        frags_transpose<true, 4, 8>(scr, wk);
        frags_transpose<false, 0, 8>(scr, wv);
    }
#pragma unroll
    for (int i = 0; i < 2; ++i) {
        const int idx = tid + i * 256;
        const int row = idx >> 6, ch = idx & 63;
        const u32x4 v = *(const u32x4*)(mem0 + row * 512 + ch * 8);
        *(u32x4*)(l_mem + (ch >> 2) * 512 + ((ch & 3) * 8 + row) * 16) = v;
    }
    const unsigned kfo = (unsigned)(OFF_KF + ((size_t)((h * 4 + wave) * 2) * 64 + lane) * 16);
#define LD_DEV(off) __builtin_bit_cast(bf16x8, __builtin_amdgcn_raw_buffer_load_b128(wrs, (off), 0, 16))
    const unsigned kmo = (unsigned)(OFF_KM + ((size_t)(h * 4 + wave) * 64 + lane) * 16);
    const unsigned vmo = (unsigned)(OFF_VM + ((size_t)((h * 4 + wave) * 2) * 64 + lane) * 8);
    const int mrd = (g * 8 + (c & 7)) * 16;
    const int srd = (c >> 3) * 1024 + (g * 16 + (c & 7)) * 16;
    const int srd2 = 2048 + ((g & 1) * 16 + (c & 7)) * 16 + (2 * (g >> 1) + (c >> 3)) * 4;
    const unsigned poff = c < 8 ? (unsigned)((((part * (32 / CPH) + wave * TPW) * 4 + (g >> 1) * 2 + (c >> 2)) * 32 + h) * 128 + ((c & 3) * 2 + (g & 1)) * 16) : 0x40000000u;
    unsigned cap = SPIN_CAP;
    wait_word(wrs, (unsigned)(OFF_CNT + 6 * 128), 64, cap);
    bf16x8 kf0 = LD_DEV(kfo), kf1 = LD_DEV(kfo + 1024);
    bf16x8 vf0 = LD_DEV(kfo + 16 * MB), vf1 = LD_DEV(kfo + 16 * MB + 1024);
    __syncthreads();
    unsigned kvv = 0;
#define VACC0(ks) (((ks) & 1) ? aw0 : av0)
#define VACC1(ks) (((ks) & 1) ? aw1 : av1)
#define VSTEP(ks) do { MFMA_VV(VACC0(ks), mf[(ks) & 7], wv[0][ks]); MFMA_VV(VACC1(ks), mf[(ks) & 7], wv[1][ks]); \
                       if ((ks) < 8) mf[(ks) & 7] = *(const bf16x8*)(l_mem + ((ks) + 8) * 512 + mrd); } while (0)
    for (int s = 0; s < NSEG_; ++s) {
        if (s == NSEG_ - 1 && part == 0 && wave == 0)
            __builtin_amdgcn_raw_buffer_store_b32((unsigned)s, wrs, get_lane() == 0 ? PROG + h * 128 : 0x40000000u, 0, 16);
        f32x4 aq0, aq1, ak0, ak1, av0, av1, aw0, aw1;
        bf16x8 mf[8];
#pragma unroll
        for (int ks = 0; ks < 8; ++ks) mf[ks] = *(const bf16x8*)(l_mem + ks * 512 + mrd);
#pragma unroll
        for (int ks = 0; ks < 16; ++ks) {
            const bf16x8 m = mf[ks & 7];
            if (ks == 0) { MFMA_AA0(aq0, wq[0][ks], m); MFMA_AA0(aq1, wq[1][ks], m); MFMA_AA0(ak0, wk[0][ks], m); MFMA_AA0(ak1, wk[1][ks], m); }
            else { MFMA_AA(aq0, wq[0][ks], m); MFMA_AA(aq1, wq[1][ks], m); MFMA_AA(ak0, wk[0][ks], m); MFMA_AA(ak1, wk[1][ks], m); }
            mf[ks & 7] = *(const bf16x8*)(l_mem + ((ks + 8) & 15) * 512 + mrd);
        }
        MFMA_VV0(av0, mf[0], wv[0][0]); MFMA_VV0(av1, mf[0], wv[1][0]);
        mf[0] = *(const bf16x8*)(l_mem + 8 * 512 + mrd);
        asm volatile("" : "+v"(aq0), "+v"(aq1), "+v"(ak0), "+v"(ak1));
        bf16x8 qB = pack8(aq0, aq1);
        bf16x8 kA = pack8(ak0, ak1);
        NOP_IN2(qB, kA);
        f32x4 s0, s1, s2;
        MFMA_VV0(s0, kf0, qB); MFMA_VV0(s1, kf1, qB); MFMA_VV0(s2, kA, qB);
        MFMA_VV0(aw0, mf[1], wv[0][1]); MFMA_VV0(aw1, mf[1], wv[1][1]);
        mf[1] = *(const bf16x8*)(l_mem + 9 * 512 + mrd);
        asm volatile("" : "+v"(s0), "+v"(s1), "+v"(s2));
        *(f32x4*)(l_sx + (wave * 3 + 0) * 1024 + lane * 16) = s0;
        *(f32x4*)(l_sx + (wave * 3 + 1) * 1024 + lane * 16) = s1;
        *(f32x4*)(l_sx + (wave * 3 + 2) * 1024 + lane * 16) = s2;
        VSTEP(2);
        __syncthreads();
        f32x4 sa = *(const f32x4*)(l_sx + 0 * 3072 + srd), sb = *(const f32x4*)(l_sx + 1 * 3072 + srd);
        f32x4 sc = *(const f32x4*)(l_sx + 2 * 3072 + srd), sd = *(const f32x4*)(l_sx + 3 * 3072 + srd);
        float ma = *(const float*)(l_sx + 0 * 3072 + srd2), mb = *(const float*)(l_sx + 1 * 3072 + srd2);
        float mc = *(const float*)(l_sx + 2 * 3072 + srd2), md = *(const float*)(l_sx + 3 * 3072 + srd2);
        VSTEP(3); VSTEP(4); VSTEP(5); VSTEP(6); VSTEP(7); VSTEP(8); VSTEP(9);
        VSTEP(10); VSTEP(11); VSTEP(12); VSTEP(13); VSTEP(14); VSTEP(15);
#define MAX2(a, b) __builtin_amdgcn_fmed3f(a, b, __builtin_inff())
        __builtin_amdgcn_sched_barrier(0);
        f32x4 e = (sa + sb) + (sc + sd);
        float em = (ma + mb) + (mc + md);
        unsigned r01 = pack2(e[0], e[1]), r23 = pack2(e[2], e[3]), rm = pack2(em, em);
        e[0] = bflo(r01) * SCALE_; e[1] = bfhi(r01) * SCALE_; e[2] = bflo(r23) * SCALE_; e[3] = bfhi(r23) * SCALE_; em = bflo(rm) * SCALE_;
        r01 = pack2(e[0], e[1]); r23 = pack2(e[2], e[3]); rm = pack2(em, em);
        e[0] = bflo(r01); e[1] = bfhi(r01); e[2] = bflo(r23); e[3] = bfhi(r23); em = bflo(rm);
        float mx = MAX2(MAX2(MAX2(e[0], e[1]), MAX2(e[2], e[3])), em);
        mx = MAX2(mx, __uint_as_float(ror8(__float_as_uint(mx))));
        { auto r = __builtin_amdgcn_permlane16_swap(__float_as_uint(mx), __float_as_uint(mx), false, false);
          mx = MAX2(__uint_as_float(r[0]), __uint_as_float(r[1])); }
        { auto r = __builtin_amdgcn_permlane32_swap(__float_as_uint(mx), __float_as_uint(mx), false, false);
          mx = MAX2(__uint_as_float(r[0]), __uint_as_float(r[1])); }
        e[0] = __builtin_amdgcn_exp2f((e[0] - mx) * LOG2E_); e[1] = __builtin_amdgcn_exp2f((e[1] - mx) * LOG2E_);
        e[2] = __builtin_amdgcn_exp2f((e[2] - mx) * LOG2E_); e[3] = __builtin_amdgcn_exp2f((e[3] - mx) * LOG2E_);
        em = __builtin_amdgcn_exp2f((em - mx) * LOG2E_);
        const float sum = add_x32(add_x16(add_x8(((e[0] + e[1]) + (e[2] + e[3])) + em)));
        const float inv = __builtin_amdgcn_rcpf(sum);
        e *= inv; em *= inv;
        const unsigned p0 = pack2(e[0], e[1]), p1 = pack2(e[2], e[3]);
        const u32x4 pb0u = {p0, p1, ror8(p0), ror8(p1)};
        const unsigned dm = pack2(em, __uint_as_float(ror8(__float_as_uint(em))));
        auto sw = __builtin_amdgcn_permlane32_swap(dm, dm, false, false);
        const u32x2 pb1u = {g < 2 ? sw[0] : 0u, g < 2 ? sw[1] : 0u};
        bf16x8 pB0 = __builtin_bit_cast(bf16x8, pb0u);
        bf16x4 pB1 = __builtin_bit_cast(bf16x4, pb1u);
        NOP_OUT4(av0, av1, aw0, aw1);
        bf16x4 vA0 = pack4(av0 + aw0), vA1 = pack4(av1 + aw1);
        if (s == NSEG_ - 1) {
            if (part == 0) {
                __builtin_amdgcn_raw_buffer_store_b128(__builtin_bit_cast(u32x4, kA), wrs, kmo + s * 131072, 0, 16);
                __builtin_amdgcn_raw_buffer_store_b64(__builtin_bit_cast(u32x2, vA0), wrs, vmo + s * 131072, 0, 16);
                __builtin_amdgcn_raw_buffer_store_b64(__builtin_bit_cast(u32x2, vA1), wrs, vmo + s * 131072 + 512, 0, 16);
                asm volatile("s_waitcnt vmcnt(0)" ::: "memory");
                __syncthreads();
                if (wave == 0) __builtin_amdgcn_raw_buffer_store_b32((unsigned)NSEG_, wrs, get_lane() == 0 ? PROG + h * 128 : 0x40000000u, 0, 16);
            }
            break;
        }
        const char* wop = l_wo + wave * TPW * 4 * 1024 + lane * 16;
        constexpr int NM = TPW * 4;
        bf16x8 wf[8];
#pragma unroll
        for (int i = 0; i < 8; ++i) wf[i] = *(const bf16x8*)(wop + ((i % TPW) * 4 + (i / TPW)) * 1024);
        __builtin_amdgcn_sched_barrier(0);
        bf16x8 pB1x = ext8(pB1), vA0x = ext8(vA0), vA1x = ext8(vA1);
        asm volatile("s_nop 1" : "+v"(pB0), "+v"(pB1x), "+v"(vA0x), "+v"(vA1x));
        f32x4 o0, o1;
        MFMA_VV0(o0, vf0, pB0); MFMA_VV0(o1, vf1, pB0);
        MFMA_VV(o0, vA0x, pB1x); MFMA_VV(o1, vA1x, pB1x);
        NOP_OUT2(o0, o1);
        *(bf16x8*)(l_ox + wave * 1024 + lane * 16) = pack8(o0, o1);
        __syncthreads();
        bf16x8 oB[4];
#pragma unroll
        for (int kk = 0; kk < 4; ++kk) oB[kk] = *(const bf16x8*)(l_ox + kk * 1024 + lane * 16);
        const unsigned tag = ((s >> 1) + 1) & 1;
        const unsigned pbase = (s & 1) * 128 * 4096 + poff;
        f32x4 pa[TPW];
#pragma unroll
        for (int i = 0; i < NM; ++i) {
            const int kk = i / TPW, t = i % TPW;
            const bf16x8 w = wf[i & 7];
            if (kk == 0) MFMA_VV0(pa[t], w, oB[kk]); else MFMA_VV(pa[t], w, oB[kk]);
            if (i + 8 < NM) { const int n = i + 8; wf[i & 7] = *(const bf16x8*)(wop + ((n % TPW) * 4 + (n / TPW)) * 1024); }
        }
        NOP_OUT2(pa[TPW - 2], pa[TPW - 1]);
        {
            u32x4 pu[TPW];
#pragma unroll
            for (int q = 0; q < TPW; ++q) {
                asm volatile("" : "+v"(pa[q]));
                pu[q] = (__builtin_bit_cast(u32x4, pa[q]) & ~1u) | tag;
            }
#pragma unroll
            for (int q = 0; q < TPW; ++q) asm volatile("" : "+v"(pu[q]));
#pragma unroll
            for (int q = 0; q < TPW; ++q) __builtin_amdgcn_raw_buffer_store_b128(pu[q], prs, pbase, q * 4 * 4096, 16);
        }
        if (part == 0) {
            __builtin_amdgcn_raw_buffer_store_b128(__builtin_bit_cast(u32x4, kA), wrs, kmo + s * 131072, 0, 16);
            __builtin_amdgcn_raw_buffer_store_b64(__builtin_bit_cast(u32x2, vA0), wrs, vmo + s * 131072, 0, 16);
            __builtin_amdgcn_raw_buffer_store_b64(__builtin_bit_cast(u32x2, vA1), wrs, vmo + s * 131072 + 512, 0, 16);
        }
        if (part == 0 && wave == 0 && s > 0 && unit_of_last_seg(s - 1) >= 0)
            __builtin_amdgcn_raw_buffer_store_b32((unsigned)s, wrs, get_lane() == 0 ? PROG + h * 128 : 0x40000000u, 0, 16);
        {
            if ((s & 3) == 3 && (unsigned)__builtin_amdgcn_readfirstlane(kvv) < 64u)
                wait_word(wrs, (unsigned)(OFF_CNT + (((s + 1) >> 2) * 8 + 6) * 128), 64, cap);
            if ((s & 3) == 2) kvv = __builtin_amdgcn_raw_buffer_load_b32(wrs, (unsigned)(OFF_CNT + (((s + 2) >> 2) * 8 + 6) * 128), 0, 16);
            const unsigned so = kfo + (unsigned)(s + 1) * 32 * 8 * 1024;
            kf0 = LD_DEV(so); kf1 = LD_DEV(so + 1024);
            vf0 = LD_DEV(so + 16 * MB); vf1 = LD_DEV(so + 16 * MB + 1024);
        }
        wait_grid(gt0, GG, GG - GE - 8, GG + CHI, false);
        {
            unsigned off = (s + 1) * 65536 + (blockIdx.x % NCOPY) * 8192 + wave * 2048 + lane * 16;
            u32x4 y0, y1;
            for (unsigned spins = 0;; ++spins) {
                asm volatile("" : "+v"(off)::"memory");
                y0 = __builtin_amdgcn_raw_buffer_load_b128(mrs, off, 0, 16);
                y1 = __builtin_amdgcn_raw_buffer_load_b128(mrs, off + 1024, 0, 16);
                const unsigned mxu = max(max(max(y0[0], y0[1]), max(y0[2], y0[3])), max(max(y1[0], y1[1]), max(y1[2], y1[3])));
                const bool ok = mxu != 0xFFFFFFFFu;
                if (__all(ok)) {
                    break;
                }
                if (spins >= (unsigned)__builtin_amdgcn_readfirstlane(cap)) { cap = 0; break; }
            }
            *(u32x4*)(l_mem + wave * 2048 + lane * 16) = y0;
            *(u32x4*)(l_mem + wave * 2048 + 1024 + lane * 16) = y1;
        }
        asm volatile("s_waitcnt vmcnt(0)" ::: "memory");
        __syncthreads();
    }
}

extern "C" void kernel_launch(void* const* d_in, const int* in_sizes, int n_in, void* d_out, int out_size, void* d_ws,
                              size_t ws_size, hipStream_t stream) {
    const unsigned short* x = (const unsigned short*)d_in[0];
    const unsigned short* mem0 = (const unsigned short*)d_in[1];
    const unsigned short* Wq = (const unsigned short*)d_in[2];
    const unsigned short* Wk = (const unsigned short*)d_in[3];
    const unsigned short* Wv = (const unsigned short*)d_in[4];
    const unsigned short* Wo = (const unsigned short*)d_in[5];
    const unsigned short* Wg = (const unsigned short*)d_in[8];
    const unsigned short* Wu = (const unsigned short*)d_in[9];
    const unsigned short* Wd = (const unsigned short*)d_in[10];
    char* ws = (char*)d_ws;
    prep_kernel<<<NB_PREP, 256, 0, stream>>>(x, ws);
    chain_kernel<<<NCW + NRW + NWK, 256, 0, stream>>>(mem0, x, (unsigned short*)d_out, ws, Wg, Wu, Wd, Wq, Wk, Wv, Wo);
}
```
